```python
import torch

def _softmax(x, axis=-1):
    return torch.softmax(x, dim=axis)

def _gelu(x):
    return torch.nn.functional.gelu(x, approximate='tanh')

def _silu(x):
    return torch.nn.functional.silu(x)

def _sigmoid(x):
    return torch.sigmoid(x)

def _layernorm(x, axis=-1, eps=1e-05):
    m = x.mean(dim=axis, keepdim=True)
    v = x.var(dim=axis, keepdim=True, unbiased=False)
    return (x - m) / torch.sqrt(v + eps)

def _scatter_add(tgt, idx, src, axis=0):
    return tgt.clone().index_add_(axis, idx, src)

def _segment_sum(src, seg, n):
    out = torch.zeros((n,) + src.shape[1:], dtype=src.dtype)
    return out.index_add_(0, seg, src)

def _pad(x, pw):
    flat = tuple((v for pair in reversed(pw) for v in pair))
    return torch.nn.functional.pad(x, flat)
kernel_name = 'gated_mix__memory_aug_recurrent_mem_tr__spectral_gfnet__prompt'

def setup_inputs():

    _w = lambda a: (lambda t: t.to(torch.bfloat16) if t.is_floating_point() else t)(torch.as_tensor(a))

    def _A_setup():
        B, S, Dm = (1, 2048, 448)
        H, Dh = (16, 256)
        M, seg = (8, 32)
        seg = min(seg, S)
        S = S // seg * seg
        return {'x': _w((lambda *s: torch.randn(*s, dtype=torch.bfloat16) * 0.05)(B, S, Dm)), 'mem0': _w((lambda *s: torch.randn(*s, dtype=torch.bfloat16) * 0.4)(B, M, Dm)), 'Wq': _w((lambda *s: torch.randn(*s, dtype=torch.bfloat16) * 6.4)(Dm, H, Dh) * Dm ** (-0.5)), 'Wk': _w((lambda *s: torch.randn(*s, dtype=torch.bfloat16) * 6.4)(Dm, H, Dh) * Dm ** (-0.5)), 'Wv': _w((lambda *s: torch.randn(*s, dtype=torch.bfloat16) * 0.4)(Dm, H, Dh) * Dm ** (-0.5)), 'Wo': _w((lambda *s: torch.randn(*s, dtype=torch.bfloat16) * 0.4)(H, Dh, Dm) * (H * Dh) ** (-0.5)), 'seg_len': seg}
    _A_in = _A_setup()

    def _B_setup():
        B, S, D = (1, 2048, 448)
        Df = D // 2 + 1
        return {'x': _w((lambda *s: torch.randn(*s, dtype=torch.bfloat16) * 0.05)(B, S, D)), 'w_r': _w((lambda *s: torch.randn(*s, dtype=torch.bfloat16) * 3.2)(S, Df) * 0.02), 'w_i': _w((lambda *s: torch.randn(*s, dtype=torch.bfloat16) * 3.2)(S, Df) * 0.02)}
    _B_in = _B_setup()
    return {'A_x': _A_in['x'], 'A_mem0': _A_in['mem0'], 'A_Wq': _A_in['Wq'], 'A_Wk': _A_in['Wk'], 'A_Wv': _A_in['Wv'], 'A_Wo': _A_in['Wo'], 'A_seg_len': _A_in['seg_len'], 'B_x': _B_in['x'], 'B_w_r': _B_in['w_r'], 'B_w_i': _B_in['w_i'], 'gate_w': _w((lambda *s: torch.randn(*s, dtype=torch.bfloat16) * 0.05)(448) * 16.0)}

def reference(A_x, A_mem0, A_Wq, A_Wk, A_Wv, A_Wo, A_seg_len, B_x, B_w_r, B_w_i, gate_w):

    def _A_ref(x, mem0, Wq, Wk, Wv, Wo, seg_len):
        B, S, Dm = x.shape
        H, Dh = (Wq.shape[1], Wq.shape[2])
        M = mem0.shape[1]
        scale = Dh ** (-0.5)
        n_seg = S // seg_len
        L = M + seg_len
        idx = torch.arange(L)
        mask = (idx[:, None] < M) | (idx[:, None] >= idx[None, :])
        mem = mem0
        outs = []
        for s in range(n_seg):
            xs = x[:, s * seg_len:(s + 1) * seg_len, :].clone()
            h = torch.concatenate([mem, xs], axis=1)
            q = torch.einsum('btd,dhe->bhte', h, Wq)
            k = torch.einsum('btd,dhe->bhte', h, Wk)
            v = torch.einsum('btd,dhe->bhte', h, Wv)
            scores = torch.einsum('bhqe,bhke->bhqk', q, k) * scale
            scores = torch.where(mask[None, None], scores, -1000000000.0)
            p = _softmax(scores, axis=-1)
            o = torch.einsum('bhqk,bhke->bhqe', p, v)
            o = torch.einsum('bhte,hed->btd', o, Wo)
            mem = o[:, :M, :].clone()
            outs.append(o[:, M:, :])
        return torch.concatenate(outs, axis=1)

    def _B_ref(x, w_r, w_i):
        B, S, D = x.shape
        X = (lambda x, axes=(-2, -1): torch.fft.rfft2(x.float(), dim=axes))(x, axes=(-2, -1))
        W = w_r + 1j * w_i
        X = X * W
        y = (lambda x, s, axes=(-2, -1): torch.fft.irfft2(x, s=s, dim=axes).to(torch.bfloat16))(X, (S, D), axes=(-2, -1))
        return y
    x = A_x
    g = _sigmoid((x * gate_w).sum(-1, keepdims=True))
    a = _A_ref(x, A_mem0, A_Wq, A_Wk, A_Wv, A_Wo, A_seg_len)
    b = _B_ref(x, B_w_r, B_w_i)
    return g * a + (1 - g) * b
```

```cpp
#include <hip/hip_runtime.h>
#include <stdint.h>

#define CHAIN_XCD2 1
#define TX_DELAY 0
#define G_SLEEP 0
#define K0SC 3
#define DELAY_A 28
#define DELAY_B1 5
#define DELAY_B2 18
#define CHAIN_A_ASM \
  "s_mov_b32 s48, %0\n\t" \
  "v_mov_b32 v0, 0\n\t" \
  "ds_read_b32 v1, v0 offset:7424\n\t" \
  "ds_read_b32 v2, v0 offset:7428\n\t" \
  "ds_read_b32 v3, v0 offset:7432\n\t" \
  "ds_read_b32 v4, v0 offset:7436\n\t" \
  "ds_read_b32 v5, v0 offset:7440\n\t" \
  "ds_read_b32 v6, v0 offset:7444\n\t" \
  "ds_read_b32 v7, v0 offset:7448\n\t" \
  "ds_read_b32 v8, v0 offset:7452\n\t" \
  "s_waitcnt lgkmcnt(0)\n\t" \
  "ds_read_b32 v9, v0 offset:7456\n\t" \
  "ds_read_b32 v10, v0 offset:7460\n\t" \
  "ds_read_b32 v11, v0 offset:7464\n\t" \
  "ds_read_b32 v12, v0 offset:7468\n\t" \
  "ds_read_b32 v13, v0 offset:7472\n\t" \
  "ds_read_b32 v14, v0 offset:7476\n\t" \
  "ds_read_b32 v15, v0 offset:7480\n\t" \
  "ds_read_b32 v16, v0 offset:7484\n\t" \
  "s_waitcnt lgkmcnt(0)\n\t" \
  "ds_read_b32 v17, v0 offset:7488\n\t" \
  "ds_read_b32 v18, v0 offset:7492\n\t" \
  "s_waitcnt lgkmcnt(0)\n\t" \
  "v_readfirstlane_b32 s70, v1\n\t" \
  "v_readfirstlane_b32 s71, v2\n\t" \
  "v_readfirstlane_b32 s72, v3\n\t" \
  "v_readfirstlane_b32 s73, v4\n\t" \
  "v_readfirstlane_b32 s74, v5\n\t" \
  "v_readfirstlane_b32 s75, v6\n\t" \
  "v_readfirstlane_b32 s76, v7\n\t" \
  "v_readfirstlane_b32 s77, v8\n\t" \
  "v_readfirstlane_b32 s78, v9\n\t" \
  "v_readfirstlane_b32 s79, v10\n\t" \
  "v_readfirstlane_b32 s80, v11\n\t" \
  "v_readfirstlane_b32 s81, v12\n\t" \
  "v_readfirstlane_b32 s82, v13\n\t" \
  "v_readfirstlane_b32 s83, v14\n\t" \
  "v_readfirstlane_b32 s84, v15\n\t" \
  "v_readfirstlane_b32 s85, v16\n\t" \
  "v_readfirstlane_b32 s86, v17\n\t" \
  "v_readfirstlane_b32 s87, v18\n\t" \
  "s_nop 4\n\t" \
  "s_mov_b64 s[96:97], s[86:87]\n\t" \
  "s_and_b32 s97, s97, 0xffff\n\t" \
  "s_mov_b32 s98, 51200\n\t" \
  "s_mov_b32 s99, 0x00020000\n\t" \
  "s_mov_b32 s49, s84\n\t" \
  "s_mov_b64 s[36:37], s[74:75]\n\t" \
  "s_and_b32 s37, s37, 0xffff\n\t" \
  "s_mov_b32 s38, 4194304\n\t" \
  "s_mov_b32 s39, 0x00020000\n\t" \
  "s_mov_b64 s[40:41], s[76:77]\n\t" \
  "s_and_b32 s41, s41, 0xffff\n\t" \
  "s_mov_b32 s42, 4194304\n\t" \
  "s_mov_b32 s43, 0x00020000\n\t" \
  "s_mov_b64 s[44:45], s[72:73]\n\t" \
  "s_and_b32 s45, s45, 0xffff\n\t" \
  "s_mov_b32 s46, 1835008\n\t" \
  "s_mov_b32 s47, 0x00020000\n\t" \
  "s_mov_b64 s[52:53], s[70:71]\n\t" \
  "s_and_b32 s53, s53, 0xffff\n\t" \
  "s_mov_b32 s54, 401408\n\t" \
  "s_mov_b32 s55, 0x00020000\n\t" \
  "s_mov_b64 s[56:57], s[80:81]\n\t" \
  "s_and_b32 s57, s57, 0xffff\n\t" \
  "s_mov_b32 s58, 16777216\n\t" \
  "s_mov_b32 s59, 0x00020000\n\t" \
  "s_lshl_b32 s69, s48, 11\n\t" \
  "s_add_u32 s69, s69, s82\n\t" \
  "s_mov_b32 s50, s78\n\t" \
  "s_mov_b32 s51, s79\n\t" \
  "v_mbcnt_lo_u32_b32 v130, -1, 0\n\t" \
  "v_mbcnt_hi_u32_b32 v130, -1, v130\n\t" \
  "s_lshl_b32 s66, s48, 8\n\t" \
  "v_lshlrev_b32 v131, 2, v130\n\t" \
  "v_add_u32 v131, s66, v131\n\t" \
  "v_lshlrev_b32 v61, 4, v130\n\t" \
  "s_mul_i32 s67, s48, 100352\n\t" \
  "s_mul_i32 s68, s48, 6144\n\t" \
  "s_add_u32 s68, s68, 65536\n\t" \
  "s_add_u32 s66, s67, 57344\n\t" \
  "s_mov_b32 m0, s68\n\t" \
  "s_add_u32 s68, s68, 1024\n\t" \
  "buffer_load_dwordx4 v61, s[52:55], s66 offen lds\n\t" \
  "s_add_u32 s66, s67, 58368\n\t" \
  "s_mov_b32 m0, s68\n\t" \
  "s_add_u32 s68, s68, 1024\n\t" \
  "buffer_load_dwordx4 v61, s[52:55], s66 offen lds\n\t" \
  "s_add_u32 s66, s67, 71680\n\t" \
  "s_mov_b32 m0, s68\n\t" \
  "s_add_u32 s68, s68, 1024\n\t" \
  "buffer_load_dwordx4 v61, s[52:55], s66 offen lds\n\t" \
  "s_add_u32 s66, s67, 72704\n\t" \
  "s_mov_b32 m0, s68\n\t" \
  "s_add_u32 s68, s68, 1024\n\t" \
  "buffer_load_dwordx4 v61, s[52:55], s66 offen lds\n\t" \
  "s_add_u32 s66, s67, 86016\n\t" \
  "s_mov_b32 m0, s68\n\t" \
  "s_add_u32 s68, s68, 1024\n\t" \
  "buffer_load_dwordx4 v61, s[52:55], s66 offen lds\n\t" \
  "s_add_u32 s66, s67, 87040\n\t" \
  "s_mov_b32 m0, s68\n\t" \
  "s_add_u32 s68, s68, 1024\n\t" \
  "buffer_load_dwordx4 v61, s[52:55], s66 offen lds\n\t" \
  "s_lshl_b32 s66, s50, 5\n\t" \
  "s_lshl_b32 s68, s48, 3\n\t" \
  "s_add_u32 s66, s66, s68\n\t" \
  "s_mul_i32 s66, s66, 896\n\t" \
  "s_mul_i32 s68, s68, 1040\n\t" \
  "s_add_u32 s68, s68, 7680\n\t" \
  "s_mov_b32 m0, s68\n\t" \
  "s_add_u32 s68, s68, 1040\n\t" \
  "buffer_load_dwordx4 v61, s[44:47], s66 offen lds\n\t" \
  "s_add_u32 s66, s66, 896\n\t" \
  "s_mov_b32 m0, s68\n\t" \
  "s_add_u32 s68, s68, 1040\n\t" \
  "buffer_load_dwordx4 v61, s[44:47], s66 offen lds\n\t" \
  "s_add_u32 s66, s66, 896\n\t" \
  "s_mov_b32 m0, s68\n\t" \
  "s_add_u32 s68, s68, 1040\n\t" \
  "buffer_load_dwordx4 v61, s[44:47], s66 offen lds\n\t" \
  "s_add_u32 s66, s66, 896\n\t" \
  "s_mov_b32 m0, s68\n\t" \
  "s_add_u32 s68, s68, 1040\n\t" \
  "buffer_load_dwordx4 v61, s[44:47], s66 offen lds\n\t" \
  "s_add_u32 s66, s66, 896\n\t" \
  "s_mov_b32 m0, s68\n\t" \
  "s_add_u32 s68, s68, 1040\n\t" \
  "buffer_load_dwordx4 v61, s[44:47], s66 offen lds\n\t" \
  "s_add_u32 s66, s66, 896\n\t" \
  "s_mov_b32 m0, s68\n\t" \
  "s_add_u32 s68, s68, 1040\n\t" \
  "buffer_load_dwordx4 v61, s[44:47], s66 offen lds\n\t" \
  "s_add_u32 s66, s66, 896\n\t" \
  "s_mov_b32 m0, s68\n\t" \
  "s_add_u32 s68, s68, 1040\n\t" \
  "buffer_load_dwordx4 v61, s[44:47], s66 offen lds\n\t" \
  "s_add_u32 s66, s66, 896\n\t" \
  "s_mov_b32 m0, s68\n\t" \
  "s_add_u32 s68, s68, 1040\n\t" \
  "buffer_load_dwordx4 v61, s[44:47], s66 offen lds\n\t" \
  "s_add_u32 s66, s66, 896\n\t" \
  "s_add_u32 s66, s67, 0\n\t" \
  "buffer_load_dwordx4 a[0:3], v61, s[52:55], s66 offen\n\t" \
  "buffer_load_dwordx4 a[4:7], v61, s[52:55], s66 offen offset:1024\n\t" \
  "s_add_u32 s66, s67, 2048\n\t" \
  "buffer_load_dwordx4 a[32:35], v61, s[52:55], s66 offen\n\t" \
  "buffer_load_dwordx4 a[36:39], v61, s[52:55], s66 offen offset:1024\n\t" \
  "s_add_u32 s66, s67, 4096\n\t" \
  "buffer_load_dwordx4 a[88:91], v61, s[52:55], s66 offen\n\t" \
  "buffer_load_dwordx4 a[92:95], v61, s[52:55], s66 offen offset:1024\n\t" \
  "s_add_u32 s66, s67, 6144\n\t" \
  "buffer_load_dwordx4 a[144:147], v61, s[52:55], s66 offen\n\t" \
  "buffer_load_dwordx4 a[148:151], v61, s[52:55], s66 offen offset:1024\n\t" \
  "s_add_u32 s66, s67, 8192\n\t" \
  "buffer_load_dwordx4 a[200:203], v61, s[52:55], s66 offen\n\t" \
  "buffer_load_dwordx4 a[204:207], v61, s[52:55], s66 offen offset:1024\n\t" \
  "s_add_u32 s66, s67, 10240\n\t" \
  "buffer_load_dwordx4 v[142:145], v61, s[52:55], s66 offen\n\t" \
  "buffer_load_dwordx4 v[146:149], v61, s[52:55], s66 offen offset:1024\n\t" \
  "s_add_u32 s66, s67, 12288\n\t" \
  "buffer_load_dwordx4 v[198:201], v61, s[52:55], s66 offen\n\t" \
  "buffer_load_dwordx4 v[202:205], v61, s[52:55], s66 offen offset:1024\n\t" \
  "s_add_u32 s66, s67, 14336\n\t" \
  "buffer_load_dwordx4 a[8:11], v61, s[52:55], s66 offen\n\t" \
  "buffer_load_dwordx4 a[12:15], v61, s[52:55], s66 offen offset:1024\n\t" \
  "s_add_u32 s66, s67, 16384\n\t" \
  "buffer_load_dwordx4 a[40:43], v61, s[52:55], s66 offen\n\t" \
  "buffer_load_dwordx4 a[44:47], v61, s[52:55], s66 offen offset:1024\n\t" \
  "s_add_u32 s66, s67, 18432\n\t" \
  "buffer_load_dwordx4 a[96:99], v61, s[52:55], s66 offen\n\t" \
  "buffer_load_dwordx4 a[100:103], v61, s[52:55], s66 offen offset:1024\n\t" \
  "s_add_u32 s66, s67, 20480\n\t" \
  "buffer_load_dwordx4 a[152:155], v61, s[52:55], s66 offen\n\t" \
  "buffer_load_dwordx4 a[156:159], v61, s[52:55], s66 offen offset:1024\n\t" \
  "s_add_u32 s66, s67, 22528\n\t" \
  "buffer_load_dwordx4 a[208:211], v61, s[52:55], s66 offen\n\t" \
  "buffer_load_dwordx4 a[212:215], v61, s[52:55], s66 offen offset:1024\n\t" \
  "s_add_u32 s66, s67, 24576\n\t" \
  "buffer_load_dwordx4 v[150:153], v61, s[52:55], s66 offen\n\t" \
  "buffer_load_dwordx4 v[154:157], v61, s[52:55], s66 offen offset:1024\n\t" \
  "s_add_u32 s66, s67, 26624\n\t" \
  "buffer_load_dwordx4 v[206:209], v61, s[52:55], s66 offen\n\t" \
  "buffer_load_dwordx4 v[210:213], v61, s[52:55], s66 offen offset:1024\n\t" \
  "s_add_u32 s66, s67, 28672\n\t" \
  "buffer_load_dwordx4 a[16:19], v61, s[52:55], s66 offen\n\t" \
  "buffer_load_dwordx4 a[20:23], v61, s[52:55], s66 offen offset:1024\n\t" \
  "s_add_u32 s66, s67, 30720\n\t" \
  "buffer_load_dwordx4 a[48:51], v61, s[52:55], s66 offen\n\t" \
  "buffer_load_dwordx4 a[52:55], v61, s[52:55], s66 offen offset:1024\n\t" \
  "s_add_u32 s66, s67, 32768\n\t" \
  "buffer_load_dwordx4 a[104:107], v61, s[52:55], s66 offen\n\t" \
  "buffer_load_dwordx4 a[108:111], v61, s[52:55], s66 offen offset:1024\n\t" \
  "s_add_u32 s66, s67, 34816\n\t" \
  "buffer_load_dwordx4 a[160:163], v61, s[52:55], s66 offen\n\t" \
  "buffer_load_dwordx4 a[164:167], v61, s[52:55], s66 offen offset:1024\n\t" \
  "s_add_u32 s66, s67, 36864\n\t" \
  "buffer_load_dwordx4 a[216:219], v61, s[52:55], s66 offen\n\t" \
  "buffer_load_dwordx4 a[220:223], v61, s[52:55], s66 offen offset:1024\n\t" \
  "s_add_u32 s66, s67, 38912\n\t" \
  "buffer_load_dwordx4 v[158:161], v61, s[52:55], s66 offen\n\t" \
  "buffer_load_dwordx4 v[162:165], v61, s[52:55], s66 offen offset:1024\n\t" \
  "s_add_u32 s66, s67, 40960\n\t" \
  "buffer_load_dwordx4 v[214:217], v61, s[52:55], s66 offen\n\t" \
  "buffer_load_dwordx4 v[218:221], v61, s[52:55], s66 offen offset:1024\n\t" \
  "s_add_u32 s66, s67, 43008\n\t" \
  "buffer_load_dwordx4 a[24:27], v61, s[52:55], s66 offen\n\t" \
  "buffer_load_dwordx4 a[28:31], v61, s[52:55], s66 offen offset:1024\n\t" \
  "s_add_u32 s66, s67, 45056\n\t" \
  "buffer_load_dwordx4 a[56:59], v61, s[52:55], s66 offen\n\t" \
  "buffer_load_dwordx4 a[60:63], v61, s[52:55], s66 offen offset:1024\n\t" \
  "s_add_u32 s66, s67, 47104\n\t" \
  "buffer_load_dwordx4 a[112:115], v61, s[52:55], s66 offen\n\t" \
  "buffer_load_dwordx4 a[116:119], v61, s[52:55], s66 offen offset:1024\n\t" \
  "s_add_u32 s66, s67, 49152\n\t" \
  "buffer_load_dwordx4 a[168:171], v61, s[52:55], s66 offen\n\t" \
  "buffer_load_dwordx4 a[172:175], v61, s[52:55], s66 offen offset:1024\n\t" \
  "s_add_u32 s66, s67, 51200\n\t" \
  "buffer_load_dwordx4 a[224:227], v61, s[52:55], s66 offen\n\t" \
  "buffer_load_dwordx4 a[228:231], v61, s[52:55], s66 offen offset:1024\n\t" \
  "s_add_u32 s66, s67, 53248\n\t" \
  "buffer_load_dwordx4 v[166:169], v61, s[52:55], s66 offen\n\t" \
  "buffer_load_dwordx4 v[170:173], v61, s[52:55], s66 offen offset:1024\n\t" \
  "s_add_u32 s66, s67, 55296\n\t" \
  "buffer_load_dwordx4 v[222:225], v61, s[52:55], s66 offen\n\t" \
  "buffer_load_dwordx4 v[226:229], v61, s[52:55], s66 offen offset:1024\n\t" \
  "s_add_u32 s66, s67, 59392\n\t" \
  "buffer_load_dwordx4 a[64:67], v61, s[52:55], s66 offen\n\t" \
  "buffer_load_dwordx4 a[68:71], v61, s[52:55], s66 offen offset:1024\n\t" \
  "s_add_u32 s66, s67, 61440\n\t" \
  "buffer_load_dwordx4 a[120:123], v61, s[52:55], s66 offen\n\t" \
  "buffer_load_dwordx4 a[124:127], v61, s[52:55], s66 offen offset:1024\n\t" \
  "s_add_u32 s66, s67, 63488\n\t" \
  "buffer_load_dwordx4 a[176:179], v61, s[52:55], s66 offen\n\t" \
  "buffer_load_dwordx4 a[180:183], v61, s[52:55], s66 offen offset:1024\n\t" \
  "s_add_u32 s66, s67, 65536\n\t" \
  "buffer_load_dwordx4 a[232:235], v61, s[52:55], s66 offen\n\t" \
  "buffer_load_dwordx4 a[236:239], v61, s[52:55], s66 offen offset:1024\n\t" \
  "s_add_u32 s66, s67, 67584\n\t" \
  "buffer_load_dwordx4 v[174:177], v61, s[52:55], s66 offen\n\t" \
  "buffer_load_dwordx4 v[178:181], v61, s[52:55], s66 offen offset:1024\n\t" \
  "s_add_u32 s66, s67, 69632\n\t" \
  "buffer_load_dwordx4 v[230:233], v61, s[52:55], s66 offen\n\t" \
  "buffer_load_dwordx4 v[234:237], v61, s[52:55], s66 offen offset:1024\n\t" \
  "s_add_u32 s66, s67, 73728\n\t" \
  "buffer_load_dwordx4 a[72:75], v61, s[52:55], s66 offen\n\t" \
  "buffer_load_dwordx4 a[76:79], v61, s[52:55], s66 offen offset:1024\n\t" \
  "s_add_u32 s66, s67, 75776\n\t" \
  "buffer_load_dwordx4 a[128:131], v61, s[52:55], s66 offen\n\t" \
  "buffer_load_dwordx4 a[132:135], v61, s[52:55], s66 offen offset:1024\n\t" \
  "s_add_u32 s66, s67, 77824\n\t" \
  "buffer_load_dwordx4 a[184:187], v61, s[52:55], s66 offen\n\t" \
  "buffer_load_dwordx4 a[188:191], v61, s[52:55], s66 offen offset:1024\n\t" \
  "s_add_u32 s66, s67, 79872\n\t" \
  "buffer_load_dwordx4 a[240:243], v61, s[52:55], s66 offen\n\t" \
  "buffer_load_dwordx4 a[244:247], v61, s[52:55], s66 offen offset:1024\n\t" \
  "s_add_u32 s66, s67, 81920\n\t" \
  "buffer_load_dwordx4 v[182:185], v61, s[52:55], s66 offen\n\t" \
  "buffer_load_dwordx4 v[186:189], v61, s[52:55], s66 offen offset:1024\n\t" \
  "s_add_u32 s66, s67, 83968\n\t" \
  "buffer_load_dwordx4 v[238:241], v61, s[52:55], s66 offen\n\t" \
  "buffer_load_dwordx4 v[242:245], v61, s[52:55], s66 offen offset:1024\n\t" \
  "s_add_u32 s66, s67, 88064\n\t" \
  "buffer_load_dwordx4 a[80:83], v61, s[52:55], s66 offen\n\t" \
  "buffer_load_dwordx4 a[84:87], v61, s[52:55], s66 offen offset:1024\n\t" \
  "s_add_u32 s66, s67, 90112\n\t" \
  "buffer_load_dwordx4 a[136:139], v61, s[52:55], s66 offen\n\t" \
  "buffer_load_dwordx4 a[140:143], v61, s[52:55], s66 offen offset:1024\n\t" \
  "s_add_u32 s66, s67, 92160\n\t" \
  "buffer_load_dwordx4 a[192:195], v61, s[52:55], s66 offen\n\t" \
  "buffer_load_dwordx4 a[196:199], v61, s[52:55], s66 offen offset:1024\n\t" \
  "s_add_u32 s66, s67, 94208\n\t" \
  "buffer_load_dwordx4 a[248:251], v61, s[52:55], s66 offen\n\t" \
  "buffer_load_dwordx4 a[252:255], v61, s[52:55], s66 offen offset:1024\n\t" \
  "s_add_u32 s66, s67, 96256\n\t" \
  "buffer_load_dwordx4 v[190:193], v61, s[52:55], s66 offen\n\t" \
  "buffer_load_dwordx4 v[194:197], v61, s[52:55], s66 offen offset:1024\n\t" \
  "s_add_u32 s66, s67, 98304\n\t" \
  "buffer_load_dwordx4 v[246:249], v61, s[52:55], s66 offen\n\t" \
  "buffer_load_dwordx4 v[250:253], v61, s[52:55], s66 offen offset:1024\n\t" \
  "s_mov_b32 s66, 3211264\n\t" \
  "buffer_load_dword v62, v131, s[36:39], s66 offen\n\t" \
  "s_mov_b32 s66, 3212288\n\t" \
  "buffer_load_dword v63, v131, s[36:39], s66 offen\n\t" \
  "s_mov_b32 s66, 3213312\n\t" \
  "buffer_load_dword v116, v131, s[36:39], s66 offen\n\t" \
  "s_mov_b32 s66, 3214336\n\t" \
  "buffer_load_dword v117, v131, s[36:39], s66 offen\n\t" \
  "s_mov_b32 s66, 3215360\n\t" \
  "buffer_load_dword v118, v131, s[36:39], s66 offen\n\t" \
  "s_mov_b32 s66, 3216384\n\t" \
  "buffer_load_dword v119, v131, s[36:39], s66 offen\n\t" \
  "s_mov_b32 s66, 3217408\n\t" \
  "buffer_load_dword v120, v131, s[36:39], s66 offen\n\t" \
  "s_mov_b32 s66, 3218432\n\t" \
  "buffer_load_dword v121, v131, s[36:39], s66 offen\n\t" \
  "s_mov_b32 s66, 3219456\n\t" \
  "buffer_load_dword v122, v131, s[36:39], s66 offen\n\t" \
  "s_mov_b32 s66, 3220480\n\t" \
  "buffer_load_dword v123, v131, s[36:39], s66 offen\n\t" \
  "s_mov_b32 s66, 3221504\n\t" \
  "buffer_load_dword v124, v131, s[36:39], s66 offen\n\t" \
  "s_mov_b32 s66, 3222528\n\t" \
  "buffer_load_dword v125, v131, s[36:39], s66 offen\n\t" \
  "s_mov_b32 s66, 3223552\n\t" \
  "buffer_load_dword v126, v131, s[36:39], s66 offen\n\t" \
  "s_mov_b32 s66, 3224576\n\t" \
  "buffer_load_dword v127, v131, s[36:39], s66 offen\n\t" \
  "s_mov_b32 s66, 3225600\n\t" \
  "buffer_load_dword v128, v131, s[36:39], s66 offen\n\t" \
  "s_mov_b32 s66, 3226624\n\t" \
  "buffer_load_dword v60, v131, s[36:39], s66 offen\n\t" \
  "s_mov_b32 s66, 3227648\n\t" \
  "buffer_load_dword v56, v131, s[36:39], s66 offen\n\t" \
  "s_mov_b32 s66, 3228672\n\t" \
  "buffer_load_dword v57, v131, s[36:39], s66 offen\n\t" \
  "s_mov_b32 s66, 3229696\n\t" \
  "buffer_load_dword v58, v131, s[36:39], s66 offen\n\t" \
  "s_mov_b32 s66, 3230720\n\t" \
  "buffer_load_dword v59, v131, s[36:39], s66 offen\n\t" \
  "s_mov_b32 s66, 3239936\n\t" \
  "buffer_load_dword v129, v131, s[36:39], s66 offen\n\t" \
  "v_mov_b32 v0, 0xff800000\n\t" \
  "v_add_u32 v1, 53248, v131\n\t" \
  "ds_write_b32 v1, v0 offset:0\n\t" \
  "ds_write_b32 v1, v0 offset:1024\n\t" \
  "ds_write_b32 v1, v0 offset:2048\n\t" \
  "ds_write_b32 v1, v0 offset:3072\n\t" \
  "ds_write_b32 v1, v0 offset:4096\n\t" \
  "ds_write_b32 v1, v0 offset:5120\n\t" \
  "ds_write_b32 v1, v0 offset:6144\n\t" \
  "ds_write_b32 v1, v0 offset:7168\n\t" \
  "ds_write_b32 v1, v0 offset:8192\n\t" \
  "ds_write_b32 v1, v0 offset:9216\n\t" \
  "ds_write_b32 v1, v0 offset:10240\n\t" \
  "ds_write_b32 v1, v0 offset:11264\n\t" \
  "v_mov_b32 v2, 0\n\t" \
  "v_lshlrev_b32 v3, 2, v130\n\t" \
  "ds_write_b32 v3, v2 offset:7168\n\t" \
  "s_mov_b32 s62, 0x55555555\n\t" \
  "s_mov_b32 s63, 0x55555555\n\t" \
  "s_mov_b32 s64, 0xaaaaaaaa\n\t" \
  "s_mov_b32 s65, 0xaaaaaaaa\n\t" \
  "s_waitcnt vmcnt(0)\n\t" \
  "v_add_u32 v128, s85, v128\n\t" \
  "s_mov_b32 s52, 0\n\t" \
  "s_mov_b32 s66, 24704\n\t" \
  "PARP_%=:\n\t" \
  "buffer_load_dword v88, off, s[96:99], s66 sc1\n\t" \
  "s_waitcnt vmcnt(0)\n\t" \
  "v_readfirstlane_b32 s53, v88\n\t" \
  "s_cmp_lg_u32 s53, 0\n\t" \
  "s_cbranch_scc1 PARD_%=\n\t" \
  "s_sleep 2\n\t" \
  "s_add_u32 s52, s52, 1\n\t" \
  "s_cmp_lt_u32 s52, 65536\n\t" \
  "s_cbranch_scc1 PARP_%=\n\t" \
  "s_mov_b32 s53, 1\n\t" \
  "PARD_%=:\n\t" \
  "s_sub_u32 s53, s53, 1\n\t" \
  "s_and_b32 s54, s53, 1\n\t" \
  "s_lshr_b32 s55, s53, 1\n\t" \
  "s_and_b32 s55, s55, 1\n\t" \
  "s_sub_i32 s55, s55, s54\n\t" \
  "s_lshl_b32 s55, s55, 13\n\t" \
  "s_lshl_b32 s54, s54, 13\n\t" \
  "s_lshl_b32 s60, s50, 14\n\t" \
  "s_add_u32 s60, s60, s54\n\t" \
  "s_mov_b32 s61, s60\n\t" \
  "v_mov_b32 v88, s55\n\t" \
  "v_cmp_le_u32 vcc, 2097152, v128\n\t" \
  "s_nop 4\n\t" \
  "v_cndmask_b32 v88, 0, v88, vcc\n\t" \
  "v_add_u32 v128, v128, v88\n\t" \
  ".p2align 6\n\t" \
  "LOOP_%=:\n\t" \
  "v_mov_b32 v28, 0\n\t" \
  "v_mov_b32 v29, 0\n\t" \
  "v_mov_b32 v30, 0\n\t" \
  "v_mov_b32 v31, 0\n\t" \
  "v_mov_b32 v32, 0\n\t" \
  "v_mov_b32 v33, 0\n\t" \
  "v_mov_b32 v34, 0\n\t" \
  "v_mov_b32 v35, 0\n\t" \
  "v_mov_b32 v36, 0\n\t" \
  "v_mov_b32 v37, 0\n\t" \
  "v_mov_b32 v38, 0\n\t" \
  "v_mov_b32 v39, 0\n\t" \
  "v_mov_b32 v40, 0\n\t" \
  "v_mov_b32 v41, 0\n\t" \
  "v_mov_b32 v42, 0\n\t" \
  "v_mov_b32 v43, 0\n\t" \
  "v_mov_b32 v44, 0\n\t" \
  "v_mov_b32 v45, 0\n\t" \
  "v_mov_b32 v46, 0\n\t" \
  "v_mov_b32 v47, 0\n\t" \
  "v_mov_b32 v48, 0\n\t" \
  "v_mov_b32 v49, 0\n\t" \
  "v_mov_b32 v50, 0\n\t" \
  "v_mov_b32 v51, 0\n\t" \
  "v_mov_b32 v52, 0\n\t" \
  "v_mov_b32 v53, 0\n\t" \
  "v_mov_b32 v54, 0\n\t" \
  "v_mov_b32 v55, 0\n\t" \
  "v_mov_b32 v104, 0\n\t" \
  "v_mov_b32 v105, 0\n\t" \
  "v_mov_b32 v106, 0\n\t" \
  "v_mov_b32 v107, 0\n\t" \
  "v_mov_b32 v108, 0\n\t" \
  "v_mov_b32 v109, 0\n\t" \
  "v_mov_b32 v110, 0\n\t" \
  "v_mov_b32 v111, 0\n\t" \
  "v_mov_b32 v112, 0\n\t" \
  "v_mov_b32 v113, 0\n\t" \
  "v_mov_b32 v114, 0\n\t" \
  "v_mov_b32 v115, 0\n\t" \
  "ds_read_b128 v[64:67], v129 offset:0\n\t" \
  "ds_read_b128 v[68:71], v129 offset:1024\n\t" \
  "ds_read_b128 v[72:75], v129 offset:2048\n\t" \
  "ds_read_b128 v[76:79], v129 offset:3072\n\t" \
  "ds_read_b128 v[80:83], v129 offset:4096\n\t" \
  "ds_read_b128 v[84:87], v129 offset:5120\n\t" \
  "v_mov_b32 v102, 0\n\t" \
  "v_mov_b32 v103, 0\n\t" \
  "s_mov_b32 s66, s49\n\t" \
  "s_cmp_eq_u32 s66, 0\n\t" \
  "s_cbranch_scc1 DLYE_%=\n\t" \
  "DLY_%=:\n\t" \
  "s_sub_u32 s66, s66, 1\n\t" \
  "s_cmp_lg_u32 s66, 0\n\t" \
  "s_cbranch_scc1 DLY_%=\n\t" \
  "DLYE_%=:\n\t" \
  "s_mov_b32 s66, 0\n\t" \
  "s_memtime s[84:85]\n\t" \
  "POLL_%=:\n\t" \
  "buffer_load_dwordx4 v[88:91], v62, s[36:39], s60 offen sc1\n\t" \
  "buffer_load_dwordx4 v[92:95], v63, s[36:39], s60 offen sc1\n\t" \
  "s_waitcnt vmcnt(0)\n\t" \
  "ds_write_b128 v62, v[88:91] offset:0\n\t" \
  "ds_write_b128 v63, v[92:95] offset:0\n\t" \
  "v_max3_u32 v96, v88, v89, v90\n\t" \
  "v_max3_u32 v97, v91, v92, v93\n\t" \
  "v_max3_u32 v96, v96, v94, v95\n\t" \
  "v_max_u32 v96, v96, v97\n\t" \
  "v_cmp_eq_u32 vcc, -1, v96\n\t" \
  "s_cbranch_vccz GOT_%=\n\t" \
  "s_add_u32 s66, s66, 1\n\t" \
  "s_cmp_lt_u32 s66, 65536\n\t" \
  "s_cbranch_scc1 POLL_%=\n\t" \
  "GOT_%=:\n\t" \
  "s_memtime s[86:87]\n\t" \
  "s_waitcnt lgkmcnt(0)\n\t" \
  "s_barrier\n\t" \
  "ds_read_b128 v[0:3], v61 offset:0\n\t" \
  "ds_read_b128 v[4:7], v61 offset:1024\n\t" \
  "ds_read_b128 v[8:11], v61 offset:2048\n\t" \
  "ds_read_b128 v[12:15], v61 offset:3072\n\t" \
  "ds_read_b128 v[16:19], v61 offset:4096\n\t" \
  "ds_read_b128 v[20:23], v61 offset:5120\n\t" \
  "ds_read_b128 v[24:27], v61 offset:6144\n\t" \
  "s_sub_u32 s68, s86, s84\n\t" \
  "s_cmp_gt_u32 s68, 850\n\t" \
  "s_cselect_b32 s68, 3, -1\n\t" \
  "s_add_i32 s49, s49, s68\n\t" \
  "s_max_i32 s49, s49, 0\n\t" \
  "s_min_i32 s49, s49, 72\n\t" \
  "s_waitcnt lgkmcnt(6)\n\t" \
  "v_smfmac_f32_16x16x64_bf16 v[28:31], v[0:3], a[0:7], v60\n\t" \
  "v_smfmac_f32_16x16x64_bf16 v[32:35], v[0:3], a[8:15], v60\n\t" \
  "v_smfmac_f32_16x16x64_bf16 v[36:39], v[0:3], a[16:23], v60\n\t" \
  "v_smfmac_f32_16x16x64_bf16 v[40:43], v[0:3], a[24:31], v60\n\t" \
  "v_smfmac_f32_16x16x64_bf16 v[44:47], v[0:3], v[64:71], v60\n\t" \
  "v_smfmac_f32_16x16x64_bf16 v[48:51], v[0:3], v[72:79], v60\n\t" \
  "v_smfmac_f32_16x16x64_bf16 v[52:55], v[0:3], v[80:87], v60\n\t" \
  "s_waitcnt lgkmcnt(0)\n\t" \
  "v_smfmac_f32_16x16x64_bf16 v[28:31], v[4:7], a[32:39], v60\n\t" \
  "ds_read_b128 v[64:67], v116\n\t" \
  "v_smfmac_f32_16x16x64_bf16 v[32:35], v[4:7], a[40:47], v60\n\t" \
  "ds_read_b128 v[68:71], v116 offset:64\n\t" \
  "v_smfmac_f32_16x16x64_bf16 v[36:39], v[4:7], a[48:55], v60\n\t" \
  "ds_read_b128 v[72:75], v116 offset:16640\n\t" \
  "v_smfmac_f32_16x16x64_bf16 v[40:43], v[4:7], a[56:63], v60\n\t" \
  "ds_read_b128 v[76:79], v116 offset:16704\n\t" \
  "v_smfmac_f32_16x16x64_bf16 v[44:47], v[4:7], a[64:71], v60\n\t" \
  "v_smfmac_f32_16x16x64_bf16 v[48:51], v[4:7], a[72:79], v60\n\t" \
  "v_smfmac_f32_16x16x64_bf16 v[52:55], v[4:7], a[80:87], v60\n\t" \
  "v_smfmac_f32_16x16x64_bf16 v[28:31], v[8:11], a[88:95], v60\n\t" \
  "ds_read2_b32 v[80:81], v120 offset0:0 offset1:32\n\t" \
  "v_smfmac_f32_16x16x64_bf16 v[32:35], v[8:11], a[96:103], v60\n\t" \
  "ds_read2_b32 v[82:83], v120 offset0:1 offset1:33\n\t" \
  "v_smfmac_f32_16x16x64_bf16 v[36:39], v[8:11], a[104:111], v60\n\t" \
  "ds_read2_b32 v[84:85], v120 offset0:128 offset1:160\n\t" \
  "v_smfmac_f32_16x16x64_bf16 v[40:43], v[8:11], a[112:119], v60\n\t" \
  "ds_read2_b32 v[86:87], v120 offset0:129 offset1:161\n\t" \
  "v_smfmac_f32_16x16x64_bf16 v[44:47], v[8:11], a[120:127], v60\n\t" \
  "v_smfmac_f32_16x16x64_bf16 v[48:51], v[8:11], a[128:135], v60\n\t" \
  "v_smfmac_f32_16x16x64_bf16 v[52:55], v[8:11], a[136:143], v60\n\t" \
  "v_smfmac_f32_16x16x64_bf16 v[28:31], v[12:15], a[144:151], v60\n\t" \
  "v_smfmac_f32_16x16x64_bf16 v[32:35], v[12:15], a[152:159], v60\n\t" \
  "v_smfmac_f32_16x16x64_bf16 v[36:39], v[12:15], a[160:167], v60\n\t" \
  "v_smfmac_f32_16x16x64_bf16 v[40:43], v[12:15], a[168:175], v60\n\t" \
  "v_smfmac_f32_16x16x64_bf16 v[44:47], v[12:15], a[176:183], v60\n\t" \
  "v_smfmac_f32_16x16x64_bf16 v[48:51], v[12:15], a[184:191], v60\n\t" \
  "v_smfmac_f32_16x16x64_bf16 v[52:55], v[12:15], a[192:199], v60\n\t" \
  "v_smfmac_f32_16x16x64_bf16 v[28:31], v[16:19], a[200:207], v60\n\t" \
  "ds_read_b64 v[0:1], v117\n\t" \
  "v_smfmac_f32_16x16x64_bf16 v[32:35], v[16:19], a[208:215], v60\n\t" \
  "ds_read2_b32 v[2:3], v118 offset0:0 offset1:1\n\t" \
  "v_smfmac_f32_16x16x64_bf16 v[36:39], v[16:19], a[216:223], v60\n\t" \
  "ds_read_b64 v[4:5], v117 offset:48\n\t" \
  "v_smfmac_f32_16x16x64_bf16 v[40:43], v[16:19], a[224:231], v60\n\t" \
  "ds_read2_b32 v[6:7], v118 offset0:12 offset1:13\n\t" \
  "v_smfmac_f32_16x16x64_bf16 v[44:47], v[16:19], a[232:239], v60\n\t" \
  "v_smfmac_f32_16x16x64_bf16 v[48:51], v[16:19], a[240:247], v60\n\t" \
  "v_smfmac_f32_16x16x64_bf16 v[52:55], v[16:19], a[248:255], v60\n\t" \
  "v_smfmac_f32_16x16x64_bf16 v[28:31], v[20:23], v[142:149], v60\n\t" \
  "ds_read_b64 v[8:9], v117 offset:16640\n\t" \
  "v_smfmac_f32_16x16x64_bf16 v[32:35], v[20:23], v[150:157], v60\n\t" \
  "ds_read2_b32 v[10:11], v119 offset0:0 offset1:1\n\t" \
  "v_smfmac_f32_16x16x64_bf16 v[36:39], v[20:23], v[158:165], v60\n\t" \
  "ds_read_b64 v[12:13], v117 offset:16688\n\t" \
  "v_smfmac_f32_16x16x64_bf16 v[40:43], v[20:23], v[166:173], v60\n\t" \
  "ds_read2_b32 v[14:15], v119 offset0:12 offset1:13\n\t" \
  "v_smfmac_f32_16x16x64_bf16 v[44:47], v[20:23], v[174:181], v60\n\t" \
  "v_smfmac_f32_16x16x64_bf16 v[48:51], v[20:23], v[182:189], v60\n\t" \
  "v_smfmac_f32_16x16x64_bf16 v[52:55], v[20:23], v[190:197], v60\n\t" \
  "v_smfmac_f32_16x16x64_bf16 v[28:31], v[24:27], v[198:205], v60\n\t" \
  "ds_read2_b32 v[16:17], v121 offset0:0 offset1:32\n\t" \
  "ds_read2_b32 v[18:19], v122 offset0:0 offset1:32\n\t" \
  "v_smfmac_f32_16x16x64_bf16 v[32:35], v[24:27], v[206:213], v60\n\t" \
  "ds_read2_b32 v[20:21], v123 offset0:0 offset1:32\n\t" \
  "ds_read2_b32 v[22:23], v124 offset0:0 offset1:32\n\t" \
  "v_smfmac_f32_16x16x64_bf16 v[36:39], v[24:27], v[214:221], v60\n\t" \
  "s_nop 0\n\t" \
  "v_smfmac_f32_16x16x64_bf16 v[40:43], v[24:27], v[222:229], v60\n\t" \
  "s_nop 1\n\t" \
  "v_smfmac_f32_16x16x64_bf16 v[44:47], v[24:27], v[230:237], v60\n\t" \
  "v_cvt_pk_bf16_f32 v88, v28, v29\n\t" \
  "v_cvt_pk_bf16_f32 v89, v30, v31\n\t" \
  "v_smfmac_f32_16x16x64_bf16 v[48:51], v[24:27], v[238:245], v60\n\t" \
  "v_cvt_pk_bf16_f32 v90, v32, v33\n\t" \
  "v_cvt_pk_bf16_f32 v91, v34, v35\n\t" \
  "v_smfmac_f32_16x16x64_bf16 v[52:55], v[24:27], v[246:253], v60\n\t" \
  "v_cvt_pk_bf16_f32 v92, v36, v37\n\t" \
  "v_cvt_pk_bf16_f32 v93, v38, v39\n\t" \
  "v_mfma_f32_16x16x32_bf16 v[28:31], v[88:91], v[56:59], 0\n\t" \
  "v_cvt_pk_bf16_f32 v94, v40, v41\n\t" \
  "v_cvt_pk_bf16_f32 v95, v42, v43\n\t" \
  "v_cvt_pk_bf16_f32 v96, v44, v45\n\t" \
  "v_cvt_pk_bf16_f32 v97, v46, v47\n\t" \
  "v_mfma_f32_16x16x32_bf16 v[36:39], v[92:95], v[56:59], 0\n\t" \
  "v_cvt_pk_bf16_f32 v98, v48, v49\n\t" \
  "v_cvt_pk_bf16_f32 v99, v50, v51\n\t" \
  "v_cvt_pk_bf16_f32 v100, v52, v53\n\t" \
  "v_cvt_pk_bf16_f32 v101, v54, v55\n\t" \
  "v_mfma_f32_16x16x32_bf16 v[44:47], v[96:99], v[56:59], 0\n\t" \
  "s_nop 0\n\t" \
  "v_mfma_f32_16x16x32_bf16 v[52:55], v[100:103], v[56:59], 0\n\t" \
  "v_cvt_pk_bf16_f32 v88, v28, v29\n\t" \
  "v_cvt_pk_bf16_f32 v89, v30, v31\n\t" \
  "s_nop 1\n\t" \
  "v_cvt_pk_bf16_f32 v90, v36, v37\n\t" \
  "v_cvt_pk_bf16_f32 v91, v38, v39\n\t" \
  "s_waitcnt lgkmcnt(12)\n\t" \
  "v_cvt_pk_bf16_f32 v92, v44, v45\n\t" \
  "v_smfmac_f32_16x16x64_bf16 v[104:107], v[88:91], v[64:71], v60\n\t" \
  "v_cvt_pk_bf16_f32 v93, v46, v47\n\t" \
  "s_nop 0\n\t" \
  "v_smfmac_f32_16x16x64_bf16 v[108:111], v[88:91], v[72:79], v60\n\t" \
  "v_cvt_pk_bf16_f32 v94, v52, v53\n\t" \
  "v_cvt_pk_bf16_f32 v95, v54, v55\n\t" \
  "v_smfmac_f32_16x16x64_bf16 v[112:115], v[88:91], v[80:87], v60\n\t" \
  "s_waitcnt lgkmcnt(0)\n\t" \
  "v_smfmac_f32_16x16x64_bf16 v[104:107], v[92:95], v[0:7], v60\n\t" \
  "v_smfmac_f32_16x16x64_bf16 v[108:111], v[92:95], v[8:15], v60\n\t" \
  "v_smfmac_f32_16x16x64_bf16 v[112:115], v[92:95], v[16:23], v60\n\t" \
  "s_nop 5\n\t" \
  "ds_write2_b32 v125, v104, v105 offset0:0 offset1:48\n\t" \
  "ds_write2_b32 v125, v106, v107 offset0:96 offset1:144\n\t" \
  "ds_write2_b32 v125, v108, v109 offset0:16 offset1:64\n\t" \
  "ds_write2_b32 v125, v110, v111 offset0:112 offset1:160\n\t" \
  "ds_write2_b32 v125, v112, v113 offset0:32 offset1:80\n\t" \
  "ds_write2_b32 v125, v114, v115 offset0:128 offset1:176\n\t" \
  "s_waitcnt lgkmcnt(0)\n\t" \
  "s_barrier\n\t" \
  "ds_read2st64_b32 v[0:1], v126 offset0:0 offset1:6\n\t" \
  "ds_read2st64_b32 v[2:3], v127 offset0:0 offset1:6\n\t" \
  "ds_read2st64_b32 v[4:5], v126 offset0:12 offset1:18\n\t" \
  "ds_read2st64_b32 v[6:7], v127 offset0:12 offset1:18\n\t" \
  "ds_read2st64_b32 v[8:9], v126 offset0:24 offset1:30\n\t" \
  "ds_read2st64_b32 v[10:11], v127 offset0:24 offset1:30\n\t" \
  "ds_read2st64_b32 v[12:13], v126 offset0:36 offset1:42\n\t" \
  "ds_read2st64_b32 v[14:15], v127 offset0:36 offset1:42\n\t" \
  "s_waitcnt lgkmcnt(6)\n\t" \
  "v_add_f32 v0, v0, v1\n\t" \
  "v_add_f32 v2, v2, v3\n\t" \
  "s_waitcnt lgkmcnt(4)\n\t" \
  "v_add_f32 v4, v4, v5\n\t" \
  "v_add_f32 v6, v6, v7\n\t" \
  "v_add_f32 v0, v0, v4\n\t" \
  "v_add_f32 v2, v2, v6\n\t" \
  "s_waitcnt lgkmcnt(2)\n\t" \
  "v_add_f32 v8, v8, v9\n\t" \
  "v_add_f32 v10, v10, v11\n\t" \
  "s_waitcnt lgkmcnt(0)\n\t" \
  "v_add_f32 v12, v12, v13\n\t" \
  "v_add_f32 v14, v14, v15\n\t" \
  "v_add_f32 v8, v8, v12\n\t" \
  "v_add_f32 v10, v10, v14\n\t" \
  "v_add_f32 v0, v0, v8\n\t" \
  "v_add_f32 v2, v2, v10\n\t" \
  "v_max_f32 v4, v0, v2\n\t" \
  "s_nop 1\n\t" \
  "v_max_f32_dpp v4, v4, v4 quad_perm:[1,0,3,2] row_mask:0xf bank_mask:0xf\n\t" \
  "s_nop 1\n\t" \
  "v_max_f32_dpp v4, v4, v4 quad_perm:[2,3,0,1] row_mask:0xf bank_mask:0xf\n\t" \
  "s_nop 1\n\t" \
  "v_max_f32_dpp v4, v4, v4 row_half_mirror row_mask:0xf bank_mask:0xf\n\t" \
  "s_nop 1\n\t" \
  "v_max_f32_dpp v4, v4, v4 row_mirror row_mask:0xf bank_mask:0xf\n\t" \
  "v_mov_b32 v5, v4\n\t" \
  "s_nop 1\n\t" \
  "v_permlane16_swap_b32 v4, v5\n\t" \
  "v_max_f32 v4, v4, v5\n\t" \
  "v_sub_f32 v0, v0, v4\n\t" \
  "v_sub_f32 v2, v2, v4\n\t" \
  "v_exp_f32 v0, v0\n\t" \
  "v_exp_f32 v2, v2\n\t" \
  "s_nop 0\n\t" \
  "v_add_f32 v4, v0, v2\n\t" \
  "s_mov_b64 vcc, s[62:63]\n\t" \
  "v_cndmask_b32_dpp v6, v2, v0, vcc quad_perm:[1,0,3,2] row_mask:0xf bank_mask:0xf\n\t" \
  "v_add_f32_dpp v4, v4, v4 quad_perm:[1,0,3,2] row_mask:0xf bank_mask:0xf\n\t" \
  "s_mov_b64 vcc, s[64:65]\n\t" \
  "v_cndmask_b32_dpp v7, v0, v2, vcc quad_perm:[1,0,3,2] row_mask:0xf bank_mask:0xf\n\t" \
  "v_add_f32_dpp v4, v4, v4 quad_perm:[2,3,0,1] row_mask:0xf bank_mask:0xf\n\t" \
  "s_nop 1\n\t" \
  "v_add_f32_dpp v4, v4, v4 row_half_mirror row_mask:0xf bank_mask:0xf\n\t" \
  "s_nop 1\n\t" \
  "v_add_f32_dpp v4, v4, v4 row_mirror row_mask:0xf bank_mask:0xf\n\t" \
  "v_mov_b32 v5, v4\n\t" \
  "s_nop 1\n\t" \
  "v_permlane16_swap_b32 v4, v5\n\t" \
  "v_add_f32 v4, v4, v5\n\t" \
  "v_rcp_f32 v4, v4\n\t" \
  "s_nop 0\n\t" \
  "v_mul_f32 v6, v6, v4\n\t" \
  "v_mul_f32 v7, v7, v4\n\t" \
  "v_cvt_pk_bf16_f32 v6, v6, v7\n\t" \
  "v_min_u32 v6, -2, v6\n\t" \
  "s_nop 1\n\t" \
  "v_mov_b32_dpp v7, v6 row_shl:2 row_mask:0xf bank_mask:0xf bound_ctrl:0\n\t" \
  "v_mov_b32_dpp v8, v6 row_shl:4 row_mask:0xf bank_mask:0xf bound_ctrl:0\n\t" \
  "v_mov_b32_dpp v9, v6 row_shl:6 row_mask:0xf bank_mask:0xf bound_ctrl:0\n\t" \
  "buffer_store_dwordx4 v[6:9], v128, s[40:43], s61 offen sc1\n\t" \
  "s_add_u32 s50, s50, 1\n\t" \
  "s_add_u32 s60, s60, 16384\n\t" \
  "s_add_u32 s61, s61, 16384\n\t" \
  "s_cmp_ge_u32 s50, s51\n\t" \
  "s_cbranch_scc1 END_%=\n\t" \
  "s_lshl_b32 s66, s50, 5\n\t" \
  "s_lshl_b32 s67, s48, 3\n\t" \
  "s_add_u32 s66, s66, s67\n\t" \
  "s_mul_i32 s66, s66, 896\n\t" \
  "s_mul_i32 s67, s67, 1040\n\t" \
  "s_add_u32 s67, s67, 7680\n\t" \
  "s_mov_b32 m0, s67\n\t" \
  "s_add_u32 s67, s67, 1040\n\t" \
  "buffer_load_dwordx4 v61, s[44:47], s66 offen lds\n\t" \
  "s_add_u32 s66, s66, 896\n\t" \
  "s_mov_b32 m0, s67\n\t" \
  "s_add_u32 s67, s67, 1040\n\t" \
  "buffer_load_dwordx4 v61, s[44:47], s66 offen lds\n\t" \
  "s_add_u32 s66, s66, 896\n\t" \
  "s_mov_b32 m0, s67\n\t" \
  "s_add_u32 s67, s67, 1040\n\t" \
  "buffer_load_dwordx4 v61, s[44:47], s66 offen lds\n\t" \
  "s_add_u32 s66, s66, 896\n\t" \
  "s_mov_b32 m0, s67\n\t" \
  "s_add_u32 s67, s67, 1040\n\t" \
  "buffer_load_dwordx4 v61, s[44:47], s66 offen lds\n\t" \
  "s_add_u32 s66, s66, 896\n\t" \
  "s_mov_b32 m0, s67\n\t" \
  "s_add_u32 s67, s67, 1040\n\t" \
  "buffer_load_dwordx4 v61, s[44:47], s66 offen lds\n\t" \
  "s_add_u32 s66, s66, 896\n\t" \
  "s_mov_b32 m0, s67\n\t" \
  "s_add_u32 s67, s67, 1040\n\t" \
  "buffer_load_dwordx4 v61, s[44:47], s66 offen lds\n\t" \
  "s_add_u32 s66, s66, 896\n\t" \
  "s_mov_b32 m0, s67\n\t" \
  "s_add_u32 s67, s67, 1040\n\t" \
  "buffer_load_dwordx4 v61, s[44:47], s66 offen lds\n\t" \
  "s_add_u32 s66, s66, 896\n\t" \
  "s_mov_b32 m0, s67\n\t" \
  "s_add_u32 s67, s67, 1040\n\t" \
  "buffer_load_dwordx4 v61, s[44:47], s66 offen lds\n\t" \
  "s_add_u32 s66, s66, 896\n\t" \
  "s_branch LOOP_%=\n\t" \
  "END_%=:\n\t" \
  "s_waitcnt vmcnt(0) lgkmcnt(0)\n\t" \
  "s_endpgm\n\t" \
  "CEND_%=:\n\t" \
  ".Lcend_A:\n\t"
#define CHAIN_B_ASM \
  "s_mov_b32 s48, %0\n\t" \
  "v_mov_b32 v0, 0\n\t" \
  "ds_read_b32 v1, v0 offset:7168\n\t" \
  "ds_read_b32 v2, v0 offset:7172\n\t" \
  "ds_read_b32 v3, v0 offset:7176\n\t" \
  "ds_read_b32 v4, v0 offset:7180\n\t" \
  "ds_read_b32 v5, v0 offset:7184\n\t" \
  "ds_read_b32 v6, v0 offset:7188\n\t" \
  "ds_read_b32 v7, v0 offset:7192\n\t" \
  "ds_read_b32 v8, v0 offset:7196\n\t" \
  "s_waitcnt lgkmcnt(0)\n\t" \
  "ds_read_b32 v9, v0 offset:7200\n\t" \
  "ds_read_b32 v10, v0 offset:7204\n\t" \
  "ds_read_b32 v11, v0 offset:7208\n\t" \
  "ds_read_b32 v12, v0 offset:7212\n\t" \
  "ds_read_b32 v13, v0 offset:7216\n\t" \
  "ds_read_b32 v14, v0 offset:7220\n\t" \
  "ds_read_b32 v15, v0 offset:7224\n\t" \
  "ds_read_b32 v16, v0 offset:7228\n\t" \
  "s_waitcnt lgkmcnt(0)\n\t" \
  "ds_read_b32 v17, v0 offset:7232\n\t" \
  "ds_read_b32 v18, v0 offset:7236\n\t" \
  "ds_read_b32 v19, v0 offset:7240\n\t" \
  "ds_read_b32 v20, v0 offset:7244\n\t" \
  "ds_read_b32 v21, v0 offset:7248\n\t" \
  "ds_read_b32 v22, v0 offset:7252\n\t" \
  "s_waitcnt lgkmcnt(0)\n\t" \
  "v_readfirstlane_b32 s70, v1\n\t" \
  "v_readfirstlane_b32 s71, v2\n\t" \
  "v_readfirstlane_b32 s72, v3\n\t" \
  "v_readfirstlane_b32 s73, v4\n\t" \
  "v_readfirstlane_b32 s74, v5\n\t" \
  "v_readfirstlane_b32 s75, v6\n\t" \
  "v_readfirstlane_b32 s76, v7\n\t" \
  "v_readfirstlane_b32 s77, v8\n\t" \
  "v_readfirstlane_b32 s78, v9\n\t" \
  "v_readfirstlane_b32 s79, v10\n\t" \
  "v_readfirstlane_b32 s80, v11\n\t" \
  "v_readfirstlane_b32 s81, v12\n\t" \
  "v_readfirstlane_b32 s82, v13\n\t" \
  "v_readfirstlane_b32 s83, v14\n\t" \
  "v_readfirstlane_b32 s84, v15\n\t" \
  "v_readfirstlane_b32 s85, v16\n\t" \
  "v_readfirstlane_b32 s86, v17\n\t" \
  "v_readfirstlane_b32 s87, v18\n\t" \
  "v_readfirstlane_b32 s88, v19\n\t" \
  "v_readfirstlane_b32 s89, v20\n\t" \
  "v_readfirstlane_b32 s90, v21\n\t" \
  "v_readfirstlane_b32 s91, v22\n\t" \
  "s_nop 4\n\t" \
  "s_mov_b32 s65, s88\n\t" \
  "s_mov_b32 s49, s89\n\t" \
  "s_mov_b64 s[96:97], s[90:91]\n\t" \
  "s_and_b32 s97, s97, 0xffff\n\t" \
  "s_mov_b32 s98, 51200\n\t" \
  "s_mov_b32 s99, 0x00020000\n\t" \
  "s_mov_b64 s[36:37], s[74:75]\n\t" \
  "s_and_b32 s37, s37, 0xffff\n\t" \
  "s_mov_b32 s38, 4194304\n\t" \
  "s_mov_b32 s39, 0x00020000\n\t" \
  "s_mov_b64 s[40:41], s[76:77]\n\t" \
  "s_and_b32 s41, s41, 0xffff\n\t" \
  "s_mov_b32 s42, 4194304\n\t" \
  "s_mov_b32 s43, 0x00020000\n\t" \
  "s_mov_b64 s[44:45], s[72:73]\n\t" \
  "s_and_b32 s45, s45, 0xffff\n\t" \
  "s_mov_b32 s46, 29360128\n\t" \
  "s_mov_b32 s47, 0x00020000\n\t" \
  "s_mul_i32 s66, s48, 1605632\n\t" \
  "s_add_u32 s70, s70, s66\n\t" \
  "s_addc_u32 s71, s71, 0\n\t" \
  "s_mov_b64 s[52:53], s[70:71]\n\t" \
  "s_and_b32 s53, s53, 0xffff\n\t" \
  "s_mov_b32 s54, 1605632\n\t" \
  "s_mov_b32 s55, 0x00020000\n\t" \
  "s_mov_b32 s64, s86\n\t" \
  "s_mov_b64 s[92:93], s[84:85]\n\t" \
  "s_and_b32 s93, s93, 0xffff\n\t" \
  "s_mov_b32 s94, 7340032\n\t" \
  "s_mov_b32 s95, 0x00020000\n\t" \
  "s_mov_b64 s[56:57], s[80:81]\n\t" \
  "s_and_b32 s57, s57, 0xffff\n\t" \
  "s_mov_b32 s58, 16777216\n\t" \
  "s_mov_b32 s59, 0x00020000\n\t" \
  "s_lshl_b32 s69, s48, 11\n\t" \
  "s_add_u32 s69, s69, s82\n\t" \
  "s_mov_b32 s50, s78\n\t" \
  "s_mov_b32 s51, s79\n\t" \
  "v_mbcnt_lo_u32_b32 v162, -1, 0\n\t" \
  "v_mbcnt_hi_u32_b32 v162, -1, v162\n\t" \
  "s_lshl_b32 s66, s48, 8\n\t" \
  "v_lshlrev_b32 v163, 2, v162\n\t" \
  "v_add_u32 v163, s66, v163\n\t" \
  "v_lshlrev_b32 v164, 4, v162\n\t" \
  "s_mul_i32 s68, s48, 28672\n\t" \
  "s_add_u32 s68, s68, 44288\n\t" \
  "s_mov_b32 s66, 1204224\n\t" \
  "s_mov_b32 m0, s68\n\t" \
  "s_add_u32 s68, s68, 1024\n\t" \
  "buffer_load_dwordx4 v164, s[52:55], s66 offen lds\n\t" \
  "s_mov_b32 s66, 1205248\n\t" \
  "s_mov_b32 m0, s68\n\t" \
  "s_add_u32 s68, s68, 1024\n\t" \
  "buffer_load_dwordx4 v164, s[52:55], s66 offen lds\n\t" \
  "s_mov_b32 s66, 1206272\n\t" \
  "s_mov_b32 m0, s68\n\t" \
  "s_add_u32 s68, s68, 1024\n\t" \
  "buffer_load_dwordx4 v164, s[52:55], s66 offen lds\n\t" \
  "s_mov_b32 s66, 1207296\n\t" \
  "s_mov_b32 m0, s68\n\t" \
  "s_add_u32 s68, s68, 1024\n\t" \
  "buffer_load_dwordx4 v164, s[52:55], s66 offen lds\n\t" \
  "s_mov_b32 s66, 1208320\n\t" \
  "s_mov_b32 m0, s68\n\t" \
  "s_add_u32 s68, s68, 1024\n\t" \
  "buffer_load_dwordx4 v164, s[52:55], s66 offen lds\n\t" \
  "s_mov_b32 s66, 1209344\n\t" \
  "s_mov_b32 m0, s68\n\t" \
  "s_add_u32 s68, s68, 1024\n\t" \
  "buffer_load_dwordx4 v164, s[52:55], s66 offen lds\n\t" \
  "s_mov_b32 s66, 1210368\n\t" \
  "s_mov_b32 m0, s68\n\t" \
  "s_add_u32 s68, s68, 1024\n\t" \
  "buffer_load_dwordx4 v164, s[52:55], s66 offen lds\n\t" \
  "s_mov_b32 s66, 1211392\n\t" \
  "s_mov_b32 m0, s68\n\t" \
  "s_add_u32 s68, s68, 1024\n\t" \
  "buffer_load_dwordx4 v164, s[52:55], s66 offen lds\n\t" \
  "s_mov_b32 s66, 1212416\n\t" \
  "s_mov_b32 m0, s68\n\t" \
  "s_add_u32 s68, s68, 1024\n\t" \
  "buffer_load_dwordx4 v164, s[52:55], s66 offen lds\n\t" \
  "s_mov_b32 s66, 1213440\n\t" \
  "s_mov_b32 m0, s68\n\t" \
  "s_add_u32 s68, s68, 1024\n\t" \
  "buffer_load_dwordx4 v164, s[52:55], s66 offen lds\n\t" \
  "s_mov_b32 s66, 1214464\n\t" \
  "s_mov_b32 m0, s68\n\t" \
  "s_add_u32 s68, s68, 1024\n\t" \
  "buffer_load_dwordx4 v164, s[52:55], s66 offen lds\n\t" \
  "s_mov_b32 s66, 1215488\n\t" \
  "s_mov_b32 m0, s68\n\t" \
  "s_add_u32 s68, s68, 1024\n\t" \
  "buffer_load_dwordx4 v164, s[52:55], s66 offen lds\n\t" \
  "s_mov_b32 s66, 1216512\n\t" \
  "s_mov_b32 m0, s68\n\t" \
  "s_add_u32 s68, s68, 1024\n\t" \
  "buffer_load_dwordx4 v164, s[52:55], s66 offen lds\n\t" \
  "s_mov_b32 s66, 1217536\n\t" \
  "s_mov_b32 m0, s68\n\t" \
  "s_add_u32 s68, s68, 1024\n\t" \
  "buffer_load_dwordx4 v164, s[52:55], s66 offen lds\n\t" \
  "s_mov_b32 s66, 1218560\n\t" \
  "s_mov_b32 m0, s68\n\t" \
  "s_add_u32 s68, s68, 1024\n\t" \
  "buffer_load_dwordx4 v164, s[52:55], s66 offen lds\n\t" \
  "s_mov_b32 s66, 1219584\n\t" \
  "s_mov_b32 m0, s68\n\t" \
  "s_add_u32 s68, s68, 1024\n\t" \
  "buffer_load_dwordx4 v164, s[52:55], s66 offen lds\n\t" \
  "s_mov_b32 s66, 1220608\n\t" \
  "s_mov_b32 m0, s68\n\t" \
  "s_add_u32 s68, s68, 1024\n\t" \
  "buffer_load_dwordx4 v164, s[52:55], s66 offen lds\n\t" \
  "s_mov_b32 s66, 1221632\n\t" \
  "s_mov_b32 m0, s68\n\t" \
  "s_add_u32 s68, s68, 1024\n\t" \
  "buffer_load_dwordx4 v164, s[52:55], s66 offen lds\n\t" \
  "s_mov_b32 s66, 1222656\n\t" \
  "s_mov_b32 m0, s68\n\t" \
  "s_add_u32 s68, s68, 1024\n\t" \
  "buffer_load_dwordx4 v164, s[52:55], s66 offen lds\n\t" \
  "s_mov_b32 s66, 1223680\n\t" \
  "s_mov_b32 m0, s68\n\t" \
  "s_add_u32 s68, s68, 1024\n\t" \
  "buffer_load_dwordx4 v164, s[52:55], s66 offen lds\n\t" \
  "s_mov_b32 s66, 1224704\n\t" \
  "s_mov_b32 m0, s68\n\t" \
  "s_add_u32 s68, s68, 1024\n\t" \
  "buffer_load_dwordx4 v164, s[52:55], s66 offen lds\n\t" \
  "s_mov_b32 s66, 1225728\n\t" \
  "s_mov_b32 m0, s68\n\t" \
  "s_add_u32 s68, s68, 1024\n\t" \
  "buffer_load_dwordx4 v164, s[52:55], s66 offen lds\n\t" \
  "s_mov_b32 s66, 1226752\n\t" \
  "s_mov_b32 m0, s68\n\t" \
  "s_add_u32 s68, s68, 1024\n\t" \
  "buffer_load_dwordx4 v164, s[52:55], s66 offen lds\n\t" \
  "s_mov_b32 s66, 1227776\n\t" \
  "s_mov_b32 m0, s68\n\t" \
  "s_add_u32 s68, s68, 1024\n\t" \
  "buffer_load_dwordx4 v164, s[52:55], s66 offen lds\n\t" \
  "s_mov_b32 s66, 1228800\n\t" \
  "s_mov_b32 m0, s68\n\t" \
  "s_add_u32 s68, s68, 1024\n\t" \
  "buffer_load_dwordx4 v164, s[52:55], s66 offen lds\n\t" \
  "s_mov_b32 s66, 1229824\n\t" \
  "s_mov_b32 m0, s68\n\t" \
  "s_add_u32 s68, s68, 1024\n\t" \
  "buffer_load_dwordx4 v164, s[52:55], s66 offen lds\n\t" \
  "s_mov_b32 s66, 1230848\n\t" \
  "s_mov_b32 m0, s68\n\t" \
  "s_add_u32 s68, s68, 1024\n\t" \
  "buffer_load_dwordx4 v164, s[52:55], s66 offen lds\n\t" \
  "s_mov_b32 s66, 1231872\n\t" \
  "s_mov_b32 m0, s68\n\t" \
  "s_add_u32 s68, s68, 1024\n\t" \
  "buffer_load_dwordx4 v164, s[52:55], s66 offen lds\n\t" \
  "s_mov_b32 s66, 0\n\t" \
  "buffer_load_dwordx4 a[0:3], v164, s[52:55], s66 offen\n\t" \
  "buffer_load_dwordx4 a[4:7], v164, s[52:55], s66 offen offset:1024\n\t" \
  "s_mov_b32 s66, 2048\n\t" \
  "buffer_load_dwordx4 a[8:11], v164, s[52:55], s66 offen\n\t" \
  "buffer_load_dwordx4 a[12:15], v164, s[52:55], s66 offen offset:1024\n\t" \
  "s_mov_b32 s66, 4096\n\t" \
  "buffer_load_dwordx4 a[16:19], v164, s[52:55], s66 offen\n\t" \
  "buffer_load_dwordx4 a[20:23], v164, s[52:55], s66 offen offset:1024\n\t" \
  "s_mov_b32 s66, 6144\n\t" \
  "buffer_load_dwordx4 a[24:27], v164, s[52:55], s66 offen\n\t" \
  "buffer_load_dwordx4 a[28:31], v164, s[52:55], s66 offen offset:1024\n\t" \
  "s_mov_b32 s66, 8192\n\t" \
  "buffer_load_dwordx4 a[32:35], v164, s[52:55], s66 offen\n\t" \
  "buffer_load_dwordx4 a[36:39], v164, s[52:55], s66 offen offset:1024\n\t" \
  "s_mov_b32 s66, 10240\n\t" \
  "buffer_load_dwordx4 a[40:43], v164, s[52:55], s66 offen\n\t" \
  "buffer_load_dwordx4 a[44:47], v164, s[52:55], s66 offen offset:1024\n\t" \
  "s_mov_b32 s66, 12288\n\t" \
  "buffer_load_dwordx4 a[48:51], v164, s[52:55], s66 offen\n\t" \
  "buffer_load_dwordx4 a[52:55], v164, s[52:55], s66 offen offset:1024\n\t" \
  "s_mov_b32 s66, 14336\n\t" \
  "buffer_load_dwordx4 a[56:59], v164, s[52:55], s66 offen\n\t" \
  "buffer_load_dwordx4 a[60:63], v164, s[52:55], s66 offen offset:1024\n\t" \
  "s_mov_b32 s66, 16384\n\t" \
  "buffer_load_dwordx4 a[64:67], v164, s[52:55], s66 offen\n\t" \
  "buffer_load_dwordx4 a[68:71], v164, s[52:55], s66 offen offset:1024\n\t" \
  "s_mov_b32 s66, 18432\n\t" \
  "buffer_load_dwordx4 a[72:75], v164, s[52:55], s66 offen\n\t" \
  "buffer_load_dwordx4 a[76:79], v164, s[52:55], s66 offen offset:1024\n\t" \
  "s_mov_b32 s66, 20480\n\t" \
  "buffer_load_dwordx4 a[80:83], v164, s[52:55], s66 offen\n\t" \
  "buffer_load_dwordx4 a[84:87], v164, s[52:55], s66 offen offset:1024\n\t" \
  "s_mov_b32 s66, 22528\n\t" \
  "buffer_load_dwordx4 a[88:91], v164, s[52:55], s66 offen\n\t" \
  "buffer_load_dwordx4 a[92:95], v164, s[52:55], s66 offen offset:1024\n\t" \
  "s_mov_b32 s66, 24576\n\t" \
  "buffer_load_dwordx4 a[96:99], v164, s[52:55], s66 offen\n\t" \
  "buffer_load_dwordx4 a[100:103], v164, s[52:55], s66 offen offset:1024\n\t" \
  "s_mov_b32 s66, 26624\n\t" \
  "buffer_load_dwordx4 a[104:107], v164, s[52:55], s66 offen\n\t" \
  "buffer_load_dwordx4 a[108:111], v164, s[52:55], s66 offen offset:1024\n\t" \
  "s_mov_b32 s66, 401408\n\t" \
  "buffer_load_dwordx4 a[112:115], v164, s[52:55], s66 offen\n\t" \
  "buffer_load_dwordx4 a[116:119], v164, s[52:55], s66 offen offset:1024\n\t" \
  "s_mov_b32 s66, 403456\n\t" \
  "buffer_load_dwordx4 a[120:123], v164, s[52:55], s66 offen\n\t" \
  "buffer_load_dwordx4 a[124:127], v164, s[52:55], s66 offen offset:1024\n\t" \
  "s_mov_b32 s66, 405504\n\t" \
  "buffer_load_dwordx4 a[128:131], v164, s[52:55], s66 offen\n\t" \
  "buffer_load_dwordx4 a[132:135], v164, s[52:55], s66 offen offset:1024\n\t" \
  "s_mov_b32 s66, 407552\n\t" \
  "buffer_load_dwordx4 a[136:139], v164, s[52:55], s66 offen\n\t" \
  "buffer_load_dwordx4 a[140:143], v164, s[52:55], s66 offen offset:1024\n\t" \
  "s_mov_b32 s66, 409600\n\t" \
  "buffer_load_dwordx4 a[144:147], v164, s[52:55], s66 offen\n\t" \
  "buffer_load_dwordx4 a[148:151], v164, s[52:55], s66 offen offset:1024\n\t" \
  "s_mov_b32 s66, 411648\n\t" \
  "buffer_load_dwordx4 a[152:155], v164, s[52:55], s66 offen\n\t" \
  "buffer_load_dwordx4 a[156:159], v164, s[52:55], s66 offen offset:1024\n\t" \
  "s_mov_b32 s66, 413696\n\t" \
  "buffer_load_dwordx4 a[160:163], v164, s[52:55], s66 offen\n\t" \
  "buffer_load_dwordx4 a[164:167], v164, s[52:55], s66 offen offset:1024\n\t" \
  "s_mov_b32 s66, 415744\n\t" \
  "buffer_load_dwordx4 a[168:171], v164, s[52:55], s66 offen\n\t" \
  "buffer_load_dwordx4 a[172:175], v164, s[52:55], s66 offen offset:1024\n\t" \
  "s_mov_b32 s66, 417792\n\t" \
  "buffer_load_dwordx4 a[176:179], v164, s[52:55], s66 offen\n\t" \
  "buffer_load_dwordx4 a[180:183], v164, s[52:55], s66 offen offset:1024\n\t" \
  "s_mov_b32 s66, 419840\n\t" \
  "buffer_load_dwordx4 a[184:187], v164, s[52:55], s66 offen\n\t" \
  "buffer_load_dwordx4 a[188:191], v164, s[52:55], s66 offen offset:1024\n\t" \
  "s_mov_b32 s66, 421888\n\t" \
  "buffer_load_dwordx4 a[192:195], v164, s[52:55], s66 offen\n\t" \
  "buffer_load_dwordx4 a[196:199], v164, s[52:55], s66 offen offset:1024\n\t" \
  "s_mov_b32 s66, 423936\n\t" \
  "buffer_load_dwordx4 a[200:203], v164, s[52:55], s66 offen\n\t" \
  "buffer_load_dwordx4 a[204:207], v164, s[52:55], s66 offen offset:1024\n\t" \
  "s_mov_b32 s66, 425984\n\t" \
  "buffer_load_dwordx4 a[208:211], v164, s[52:55], s66 offen\n\t" \
  "buffer_load_dwordx4 a[212:215], v164, s[52:55], s66 offen offset:1024\n\t" \
  "s_mov_b32 s66, 428032\n\t" \
  "buffer_load_dwordx4 a[216:219], v164, s[52:55], s66 offen\n\t" \
  "buffer_load_dwordx4 a[220:223], v164, s[52:55], s66 offen offset:1024\n\t" \
  "s_mov_b32 s66, 802816\n\t" \
  "buffer_load_dwordx4 a[224:227], v164, s[52:55], s66 offen\n\t" \
  "buffer_load_dwordx4 a[228:231], v164, s[52:55], s66 offen offset:1024\n\t" \
  "s_mov_b32 s66, 804864\n\t" \
  "buffer_load_dwordx4 a[232:235], v164, s[52:55], s66 offen\n\t" \
  "buffer_load_dwordx4 a[236:239], v164, s[52:55], s66 offen offset:1024\n\t" \
  "s_mov_b32 s66, 806912\n\t" \
  "buffer_load_dwordx4 a[240:243], v164, s[52:55], s66 offen\n\t" \
  "buffer_load_dwordx4 a[244:247], v164, s[52:55], s66 offen offset:1024\n\t" \
  "s_mov_b32 s66, 808960\n\t" \
  "buffer_load_dwordx4 a[248:251], v164, s[52:55], s66 offen\n\t" \
  "buffer_load_dwordx4 a[252:255], v164, s[52:55], s66 offen offset:1024\n\t" \
  "s_mov_b32 s66, 811008\n\t" \
  "buffer_load_dwordx4 v[174:177], v164, s[52:55], s66 offen\n\t" \
  "buffer_load_dwordx4 v[178:181], v164, s[52:55], s66 offen offset:1024\n\t" \
  "s_mov_b32 s66, 813056\n\t" \
  "buffer_load_dwordx4 v[182:185], v164, s[52:55], s66 offen\n\t" \
  "buffer_load_dwordx4 v[186:189], v164, s[52:55], s66 offen offset:1024\n\t" \
  "s_mov_b32 s66, 815104\n\t" \
  "buffer_load_dwordx4 v[190:193], v164, s[52:55], s66 offen\n\t" \
  "buffer_load_dwordx4 v[194:197], v164, s[52:55], s66 offen offset:1024\n\t" \
  "s_mov_b32 s66, 817152\n\t" \
  "buffer_load_dwordx4 v[198:201], v164, s[52:55], s66 offen\n\t" \
  "buffer_load_dwordx4 v[202:205], v164, s[52:55], s66 offen offset:1024\n\t" \
  "s_mov_b32 s66, 819200\n\t" \
  "buffer_load_dwordx4 v[206:209], v164, s[52:55], s66 offen\n\t" \
  "buffer_load_dwordx4 v[210:213], v164, s[52:55], s66 offen offset:1024\n\t" \
  "s_mov_b32 s66, 821248\n\t" \
  "buffer_load_dwordx4 v[214:217], v164, s[52:55], s66 offen\n\t" \
  "buffer_load_dwordx4 v[218:221], v164, s[52:55], s66 offen offset:1024\n\t" \
  "s_mov_b32 s66, 823296\n\t" \
  "buffer_load_dwordx4 v[222:225], v164, s[52:55], s66 offen\n\t" \
  "buffer_load_dwordx4 v[226:229], v164, s[52:55], s66 offen offset:1024\n\t" \
  "s_mov_b32 s66, 825344\n\t" \
  "buffer_load_dwordx4 v[230:233], v164, s[52:55], s66 offen\n\t" \
  "buffer_load_dwordx4 v[234:237], v164, s[52:55], s66 offen offset:1024\n\t" \
  "s_mov_b32 s66, 827392\n\t" \
  "buffer_load_dwordx4 v[238:241], v164, s[52:55], s66 offen\n\t" \
  "buffer_load_dwordx4 v[242:245], v164, s[52:55], s66 offen offset:1024\n\t" \
  "s_mov_b32 s66, 829440\n\t" \
  "buffer_load_dwordx4 v[246:249], v164, s[52:55], s66 offen\n\t" \
  "buffer_load_dwordx4 v[250:253], v164, s[52:55], s66 offen offset:1024\n\t" \
  "s_mov_b32 s66, 3244032\n\t" \
  "buffer_load_dword v144, v163, s[36:39], s66 offen\n\t" \
  "s_mov_b32 s66, 3245056\n\t" \
  "buffer_load_dword v145, v163, s[36:39], s66 offen\n\t" \
  "s_mov_b32 s66, 3246080\n\t" \
  "buffer_load_dword v146, v163, s[36:39], s66 offen\n\t" \
  "s_mov_b32 s66, 3247104\n\t" \
  "buffer_load_dword v147, v163, s[36:39], s66 offen\n\t" \
  "s_mov_b32 s66, 3248128\n\t" \
  "buffer_load_dword v148, v163, s[36:39], s66 offen\n\t" \
  "s_mov_b32 s66, 3249152\n\t" \
  "buffer_load_dword v149, v163, s[36:39], s66 offen\n\t" \
  "s_mov_b32 s66, 3250176\n\t" \
  "buffer_load_dword v150, v163, s[36:39], s66 offen\n\t" \
  "s_mov_b32 s66, 3251200\n\t" \
  "buffer_load_dword v151, v163, s[36:39], s66 offen\n\t" \
  "s_mov_b32 s66, 3252224\n\t" \
  "buffer_load_dword v152, v163, s[36:39], s66 offen\n\t" \
  "s_mov_b32 s66, 3253248\n\t" \
  "buffer_load_dword v153, v163, s[36:39], s66 offen\n\t" \
  "s_mov_b32 s66, 3254272\n\t" \
  "buffer_load_dword v154, v163, s[36:39], s66 offen\n\t" \
  "s_mov_b32 s66, 3255296\n\t" \
  "buffer_load_dword v155, v163, s[36:39], s66 offen\n\t" \
  "s_mov_b32 s66, 3256320\n\t" \
  "buffer_load_dword v156, v163, s[36:39], s66 offen\n\t" \
  "s_mov_b32 s66, 3257344\n\t" \
  "buffer_load_dword v157, v163, s[36:39], s66 offen\n\t" \
  "s_mov_b32 s66, 3258368\n\t" \
  "buffer_load_dword v158, v163, s[36:39], s66 offen\n\t" \
  "s_mov_b32 s66, 3259392\n\t" \
  "buffer_load_dword v159, v163, s[36:39], s66 offen\n\t" \
  "s_mov_b32 s66, 3260416\n\t" \
  "buffer_load_dword v160, v163, s[36:39], s66 offen\n\t" \
  "s_mul_i32 s62, s50, 458752\n\t" \
  "s_mul_i32 s63, s50, 114688\n\t" \
  "s_waitcnt vmcnt(0)\n\t" \
  "v_add_u32 v156, s87, v156\n\t" \
  "s_mov_b32 s52, 0\n\t" \
  "s_mov_b32 s66, 24704\n\t" \
  "PARP_%=:\n\t" \
  "buffer_load_dword v136, off, s[96:99], s66 sc1\n\t" \
  "s_waitcnt vmcnt(0)\n\t" \
  "v_readfirstlane_b32 s53, v136\n\t" \
  "s_cmp_lg_u32 s53, 0\n\t" \
  "s_cbranch_scc1 PARD_%=\n\t" \
  "s_sleep 2\n\t" \
  "s_add_u32 s52, s52, 1\n\t" \
  "s_cmp_lt_u32 s52, 65536\n\t" \
  "s_cbranch_scc1 PARP_%=\n\t" \
  "s_mov_b32 s53, 1\n\t" \
  "PARD_%=:\n\t" \
  "s_sub_u32 s53, s53, 1\n\t" \
  "s_and_b32 s54, s53, 1\n\t" \
  "s_lshr_b32 s55, s53, 1\n\t" \
  "s_and_b32 s55, s55, 1\n\t" \
  "s_sub_i32 s55, s55, s54\n\t" \
  "s_lshl_b32 s55, s55, 13\n\t" \
  "s_lshl_b32 s54, s54, 13\n\t" \
  "s_lshl_b32 s60, s50, 14\n\t" \
  "s_add_u32 s60, s60, s54\n\t" \
  "s_mov_b32 s61, s60\n\t" \
  "v_add_u32 v151, s55, v151\n\t" \
  "v_add_u32 v152, s55, v152\n\t" \
  "s_mov_b32 s67, 0\n\t" \
  ".p2align 6\n\t" \
  "LOOP_%=:\n\t" \
  "v_mov_b32 v32, 0\n\t" \
  "v_mov_b32 v33, 0\n\t" \
  "v_mov_b32 v34, 0\n\t" \
  "v_mov_b32 v35, 0\n\t" \
  "v_mov_b32 v36, 0\n\t" \
  "v_mov_b32 v37, 0\n\t" \
  "v_mov_b32 v38, 0\n\t" \
  "v_mov_b32 v39, 0\n\t" \
  "v_mov_b32 v40, 0\n\t" \
  "v_mov_b32 v41, 0\n\t" \
  "v_mov_b32 v42, 0\n\t" \
  "v_mov_b32 v43, 0\n\t" \
  "v_mov_b32 v44, 0\n\t" \
  "v_mov_b32 v45, 0\n\t" \
  "v_mov_b32 v46, 0\n\t" \
  "v_mov_b32 v47, 0\n\t" \
  "v_mov_b32 v48, 0\n\t" \
  "v_mov_b32 v49, 0\n\t" \
  "v_mov_b32 v50, 0\n\t" \
  "v_mov_b32 v51, 0\n\t" \
  "v_mov_b32 v52, 0\n\t" \
  "v_mov_b32 v53, 0\n\t" \
  "v_mov_b32 v54, 0\n\t" \
  "v_mov_b32 v55, 0\n\t" \
  "v_mov_b32 v56, 0\n\t" \
  "v_mov_b32 v57, 0\n\t" \
  "v_mov_b32 v58, 0\n\t" \
  "v_mov_b32 v59, 0\n\t" \
  "v_mov_b32 v60, 0\n\t" \
  "v_mov_b32 v61, 0\n\t" \
  "v_mov_b32 v62, 0\n\t" \
  "v_mov_b32 v63, 0\n\t" \
  "s_cmp_lt_u32 s50, s67\n\t" \
  "s_cbranch_scc1 VXOK_%=\n\t" \
  "s_cmp_eq_u32 s50, 63\n\t" \
  "s_cbranch_scc1 VXOK_%=\n\t" \
  "s_lshl_b32 s68, s50, 7\n\t" \
  "s_mov_b32 s52, 0\n\t" \
  "VXPOLL_%=:\n\t" \
  "v_lshlrev_b32 v138, 3, v146\n\t" \
  "buffer_load_dword v136, v138, s[96:99], s68 offen sc1\n\t" \
  "s_waitcnt vmcnt(0)\n\t" \
  "v_cmp_le_u32 vcc, 28, v136\n\t" \
  "s_nop 4\n\t" \
  "s_ff0_i32_b64 s66, vcc\n\t" \
  "s_cmp_eq_i32 s66, -1\n\t" \
  "s_cselect_b32 s66, 64, s66\n\t" \
  "s_add_u32 s67, s50, s66\n\t" \
  "s_cmp_lg_u32 s66, 0\n\t" \
  "s_cbranch_scc1 VXOK_%=\n\t" \
  "s_sleep 4\n\t" \
  "s_add_u32 s52, s52, 1\n\t" \
  "s_cmp_lt_u32 s52, 32768\n\t" \
  "s_cbranch_scc1 VXPOLL_%=\n\t" \
  "VXOK_%=:\n\t" \
  "s_mov_b32 s66, s65\n\t" \
  "s_cmp_eq_u32 s66, 0\n\t" \
  "s_cbranch_scc1 DLY1E_%=\n\t" \
  "DLY1_%=:\n\t" \
  "s_sub_u32 s66, s66, 1\n\t" \
  "s_cmp_lg_u32 s66, 0\n\t" \
  "s_cbranch_scc1 DLY1_%=\n\t" \
  "DLY1E_%=:\n\t" \
  "s_mov_b32 s66, 0\n\t" \
  "s_memtime s[84:85]\n\t" \
  "POLL_%=:\n\t" \
  "buffer_load_dwordx4 v[136:139], v144, s[36:39], s60 offen sc1\n\t" \
  "buffer_load_dwordx4 v[140:143], v145, s[36:39], s60 offen sc1\n\t" \
  "s_waitcnt vmcnt(0)\n\t" \
  "ds_write_b128 v144, v[136:139] offset:0\n\t" \
  "ds_write_b128 v145, v[140:143] offset:0\n\t" \
  "v_max3_u32 v116, v136, v137, v138\n\t" \
  "v_max3_u32 v117, v139, v140, v141\n\t" \
  "v_max3_u32 v116, v116, v142, v143\n\t" \
  "v_max_u32 v116, v116, v117\n\t" \
  "v_cmp_eq_u32 vcc, -1, v116\n\t" \
  "s_cbranch_vccz GOT_%=\n\t" \
  "s_add_u32 s66, s66, 1\n\t" \
  "s_cmp_lt_u32 s66, 65536\n\t" \
  "s_cbranch_scc1 POLL_%=\n\t" \
  "GOT_%=:\n\t" \
  "s_memtime s[86:87]\n\t" \
  "s_waitcnt lgkmcnt(0)\n\t" \
  "s_barrier\n\t" \
  "ds_read_b128 v[64:67], v146 offset:0\n\t" \
  "ds_read_b128 v[68:71], v146 offset:1024\n\t" \
  "ds_read_b128 v[72:75], v146 offset:2048\n\t" \
  "ds_read_b128 v[76:79], v146 offset:3072\n\t" \
  "ds_read_b128 v[80:83], v146 offset:4096\n\t" \
  "ds_read_b128 v[84:87], v146 offset:5120\n\t" \
  "ds_read_b128 v[88:91], v146 offset:6144\n\t" \
  "s_sub_u32 s68, s86, s84\n\t" \
  "s_cmp_gt_u32 s68, 850\n\t" \
  "s_cselect_b32 s68, 3, -1\n\t" \
  "s_add_i32 s65, s65, s68\n\t" \
  "s_max_i32 s65, s65, 0\n\t" \
  "s_min_i32 s65, s65, 40\n\t" \
  "ds_read_b128 v[92:95], v148 offset:0\n\t" \
  "ds_read_b128 v[96:99], v148 offset:1024\n\t" \
  "ds_read_b128 v[100:103], v148 offset:14336\n\t" \
  "ds_read_b128 v[104:107], v148 offset:15360\n\t" \
  "ds_read_b128 v[108:111], v148 offset:2048\n\t" \
  "ds_read_b128 v[112:115], v148 offset:3072\n\t" \
  "s_waitcnt lgkmcnt(12)\n\t" \
  "v_smfmac_f32_16x16x64_bf16 v[32:35], v[64:67], a[0:7], v147\n\t" \
  "s_add_u32 s66, s62, 0\n\t" \
  "buffer_load_dwordx4 v[0:3], v157, s[44:47], s66 offen sc1\n\t" \
  "v_smfmac_f32_16x16x64_bf16 v[40:43], v[64:67], a[112:119], v147\n\t" \
  "ds_read_b128 v[116:119], v148 offset:16384\n\t" \
  "ds_read_b128 v[120:123], v148 offset:17408\n\t" \
  "s_waitcnt lgkmcnt(6)\n\t" \
  "v_smfmac_f32_16x16x64_bf16 v[56:59], v[64:67], v[92:99], v147\n\t" \
  "v_smfmac_f32_16x16x64_bf16 v[36:39], v[64:67], a[56:63], v147\n\t" \
  "buffer_load_dwordx4 v[4:7], v157, s[44:47], s66 offen offset:1024 sc1\n\t" \
  "v_smfmac_f32_16x16x64_bf16 v[44:47], v[64:67], a[168:175], v147\n\t" \
  "ds_read_b128 v[92:95], v148 offset:4096\n\t" \
  "ds_read_b128 v[96:99], v148 offset:5120\n\t" \
  "s_waitcnt lgkmcnt(6)\n\t" \
  "v_smfmac_f32_16x16x64_bf16 v[60:63], v[64:67], v[100:107], v147\n\t" \
  "v_smfmac_f32_16x16x64_bf16 v[32:35], v[68:71], a[8:15], v147\n\t" \
  "s_add_u32 s66, s62, 2048\n\t" \
  "buffer_load_dwordx4 v[8:11], v157, s[44:47], s66 offen sc1\n\t" \
  "v_smfmac_f32_16x16x64_bf16 v[40:43], v[68:71], a[120:127], v147\n\t" \
  "ds_read_b128 v[100:103], v148 offset:18432\n\t" \
  "ds_read_b128 v[104:107], v148 offset:19456\n\t" \
  "s_waitcnt lgkmcnt(6)\n\t" \
  "v_smfmac_f32_16x16x64_bf16 v[56:59], v[68:71], v[108:115], v147\n\t" \
  "v_smfmac_f32_16x16x64_bf16 v[36:39], v[68:71], a[64:71], v147\n\t" \
  "buffer_load_dwordx4 v[12:15], v157, s[44:47], s66 offen offset:1024 sc1\n\t" \
  "v_smfmac_f32_16x16x64_bf16 v[44:47], v[68:71], a[176:183], v147\n\t" \
  "ds_read_b128 v[108:111], v148 offset:6144\n\t" \
  "ds_read_b128 v[112:115], v148 offset:7168\n\t" \
  "s_waitcnt lgkmcnt(6)\n\t" \
  "v_smfmac_f32_16x16x64_bf16 v[60:63], v[68:71], v[116:123], v147\n\t" \
  "v_smfmac_f32_16x16x64_bf16 v[32:35], v[72:75], a[16:23], v147\n\t" \
  "s_add_u32 s66, s62, 57344\n\t" \
  "buffer_load_dwordx4 v[16:19], v157, s[44:47], s66 offen sc1\n\t" \
  "v_smfmac_f32_16x16x64_bf16 v[40:43], v[72:75], a[128:135], v147\n\t" \
  "ds_read_b128 v[116:119], v148 offset:20480\n\t" \
  "ds_read_b128 v[120:123], v148 offset:21504\n\t" \
  "s_waitcnt lgkmcnt(6)\n\t" \
  "v_smfmac_f32_16x16x64_bf16 v[56:59], v[72:75], v[92:99], v147\n\t" \
  "v_smfmac_f32_16x16x64_bf16 v[36:39], v[72:75], a[72:79], v147\n\t" \
  "buffer_load_dwordx4 v[20:23], v157, s[44:47], s66 offen offset:1024 sc1\n\t" \
  "v_smfmac_f32_16x16x64_bf16 v[44:47], v[72:75], a[184:191], v147\n\t" \
  "ds_read_b128 v[92:95], v148 offset:8192\n\t" \
  "ds_read_b128 v[96:99], v148 offset:9216\n\t" \
  "s_waitcnt lgkmcnt(6)\n\t" \
  "v_smfmac_f32_16x16x64_bf16 v[60:63], v[72:75], v[100:107], v147\n\t" \
  "v_smfmac_f32_16x16x64_bf16 v[32:35], v[76:79], a[24:31], v147\n\t" \
  "s_add_u32 s66, s62, 59392\n\t" \
  "buffer_load_dwordx4 v[24:27], v157, s[44:47], s66 offen sc1\n\t" \
  "v_smfmac_f32_16x16x64_bf16 v[40:43], v[76:79], a[136:143], v147\n\t" \
  "ds_read_b128 v[100:103], v148 offset:22528\n\t" \
  "ds_read_b128 v[104:107], v148 offset:23552\n\t" \
  "s_waitcnt lgkmcnt(6)\n\t" \
  "v_smfmac_f32_16x16x64_bf16 v[56:59], v[76:79], v[108:115], v147\n\t" \
  "v_smfmac_f32_16x16x64_bf16 v[36:39], v[76:79], a[80:87], v147\n\t" \
  "buffer_load_dwordx4 v[28:31], v157, s[44:47], s66 offen offset:1024 sc1\n\t" \
  "v_smfmac_f32_16x16x64_bf16 v[44:47], v[76:79], a[192:199], v147\n\t" \
  "ds_read_b128 v[108:111], v148 offset:10240\n\t" \
  "ds_read_b128 v[112:115], v148 offset:11264\n\t" \
  "s_waitcnt lgkmcnt(6)\n\t" \
  "v_smfmac_f32_16x16x64_bf16 v[60:63], v[76:79], v[116:123], v147\n\t" \
  "v_smfmac_f32_16x16x64_bf16 v[32:35], v[80:83], a[32:39], v147\n\t" \
  "s_sub_u32 s53, s63, 114688\n\t" \
  "s_max_i32 s53, s53, 0\n\t" \
  "buffer_store_dwordx4 v[166:169], v159, s[92:95], s53 offen sc1\n\t" \
  "v_smfmac_f32_16x16x64_bf16 v[40:43], v[80:83], a[144:151], v147\n\t" \
  "ds_read_b128 v[116:119], v148 offset:24576\n\t" \
  "ds_read_b128 v[120:123], v148 offset:25600\n\t" \
  "s_waitcnt lgkmcnt(6)\n\t" \
  "v_smfmac_f32_16x16x64_bf16 v[56:59], v[80:83], v[92:99], v147\n\t" \
  "v_smfmac_f32_16x16x64_bf16 v[36:39], v[80:83], a[88:95], v147\n\t" \
  "s_add_u32 s53, s53, 57344\n\t" \
  "buffer_store_dwordx4 v[170:173], v159, s[92:95], s53 offen sc1\n\t" \
  "v_smfmac_f32_16x16x64_bf16 v[44:47], v[80:83], a[200:207], v147\n\t" \
  "ds_read_b128 v[92:95], v148 offset:12288\n\t" \
  "ds_read_b128 v[96:99], v148 offset:13312\n\t" \
  "s_waitcnt lgkmcnt(6)\n\t" \
  "v_smfmac_f32_16x16x64_bf16 v[60:63], v[80:83], v[100:107], v147\n\t" \
  "v_smfmac_f32_16x16x64_bf16 v[32:35], v[84:87], a[40:47], v147\n\t" \
  "s_cmp_eq_u32 s48, 0\n\t" \
  "s_cbranch_scc0 FLX_%=\n\t" \
  "s_lshl_b32 s66, s50, 7\n\t" \
  "s_add_u32 s66, s66, 8192\n\t" \
  "v_mov_b32 v165, 1\n\t" \
  "s_mov_b64 exec, 1\n\t" \
  "s_cmp_eq_u32 s64, 0\n\t" \
  "s_cbranch_scc1 FL1_%=\n\t" \
  "buffer_atomic_add v165, off, s[96:99], s66 offset:64 sc1\n\t" \
  "FL1_%=:\n\t" \
  "s_cmp_lt_u32 s50, 2\n\t" \
  "s_cbranch_scc1 FL2_%=\n\t" \
  "s_sub_u32 s66, s66, 256\n\t" \
  "buffer_atomic_add v165, off, s[96:99], s66 sc1\n\t" \
  "FL2_%=:\n\t" \
  "s_mov_b64 exec, -1\n\t" \
  "FLX_%=:\n\t" \
  "v_smfmac_f32_16x16x64_bf16 v[40:43], v[84:87], a[152:159], v147\n\t" \
  "ds_read_b128 v[100:103], v148 offset:26624\n\t" \
  "ds_read_b128 v[104:107], v148 offset:27648\n\t" \
  "s_waitcnt lgkmcnt(6)\n\t" \
  "v_smfmac_f32_16x16x64_bf16 v[56:59], v[84:87], v[108:115], v147\n\t" \
  "v_smfmac_f32_16x16x64_bf16 v[36:39], v[84:87], a[96:103], v147\n\t" \
  "v_smfmac_f32_16x16x64_bf16 v[44:47], v[84:87], a[208:215], v147\n\t" \
  "s_waitcnt lgkmcnt(4)\n\t" \
  "v_smfmac_f32_16x16x64_bf16 v[60:63], v[84:87], v[116:123], v147\n\t" \
  "v_smfmac_f32_16x16x64_bf16 v[32:35], v[88:91], a[48:55], v147\n\t" \
  "v_smfmac_f32_16x16x64_bf16 v[40:43], v[88:91], a[160:167], v147\n\t" \
  "s_waitcnt lgkmcnt(2)\n\t" \
  "v_smfmac_f32_16x16x64_bf16 v[56:59], v[88:91], v[92:99], v147\n\t" \
  "v_smfmac_f32_16x16x64_bf16 v[36:39], v[88:91], a[104:111], v147\n\t" \
  "v_smfmac_f32_16x16x64_bf16 v[44:47], v[88:91], a[216:223], v147\n\t" \
  "s_waitcnt lgkmcnt(0)\n\t" \
  "v_smfmac_f32_16x16x64_bf16 v[60:63], v[88:91], v[100:107], v147\n\t" \
  "v_smfmac_f32_16x16x64_bf16 v[48:51], v[64:67], a[224:231], v147\n\t" \
  "v_smfmac_f32_16x16x64_bf16 v[52:55], v[64:67], v[198:205], v147\n\t" \
  "v_permlane32_swap_b32 v32, v40\n\t" \
  "v_permlane32_swap_b32 v33, v41\n\t" \
  "v_smfmac_f32_16x16x64_bf16 v[48:51], v[68:71], a[232:239], v147\n\t" \
  "v_permlane32_swap_b32 v34, v42\n\t" \
  "v_permlane32_swap_b32 v35, v43\n\t" \
  "v_smfmac_f32_16x16x64_bf16 v[52:55], v[68:71], v[206:213], v147\n\t" \
  "v_add_f32 v32, v32, v40\n\t" \
  "v_add_f32 v33, v33, v41\n\t" \
  "v_smfmac_f32_16x16x64_bf16 v[48:51], v[72:75], a[240:247], v147\n\t" \
  "v_add_f32 v34, v34, v42\n\t" \
  "v_add_f32 v35, v35, v43\n\t" \
  "v_smfmac_f32_16x16x64_bf16 v[52:55], v[72:75], v[214:221], v147\n\t" \
  "v_cvt_pk_bf16_f32 v32, v32, v33\n\t" \
  "v_cvt_pk_bf16_f32 v34, v34, v35\n\t" \
  "v_smfmac_f32_16x16x64_bf16 v[48:51], v[76:79], a[248:255], v147\n\t" \
  "ds_write2_b32 v149, v32, v34 offset0:0 offset1:2\n\t" \
  "v_permlane32_swap_b32 v36, v44\n\t" \
  "v_smfmac_f32_16x16x64_bf16 v[52:55], v[76:79], v[222:229], v147\n\t" \
  "v_permlane32_swap_b32 v37, v45\n\t" \
  "v_permlane32_swap_b32 v38, v46\n\t" \
  "v_smfmac_f32_16x16x64_bf16 v[48:51], v[80:83], v[174:181], v147\n\t" \
  "v_permlane32_swap_b32 v39, v47\n\t" \
  "v_add_f32 v36, v36, v44\n\t" \
  "v_smfmac_f32_16x16x64_bf16 v[52:55], v[80:83], v[230:237], v147\n\t" \
  "v_add_f32 v37, v37, v45\n\t" \
  "v_add_f32 v38, v38, v46\n\t" \
  "v_smfmac_f32_16x16x64_bf16 v[48:51], v[84:87], v[182:189], v147\n\t" \
  "v_add_f32 v39, v39, v47\n\t" \
  "v_cvt_pk_bf16_f32 v36, v36, v37\n\t" \
  "v_smfmac_f32_16x16x64_bf16 v[52:55], v[84:87], v[238:245], v147\n\t" \
  "v_cvt_pk_bf16_f32 v38, v38, v39\n\t" \
  "ds_write2_b32 v149, v36, v38 offset0:128 offset1:130\n\t" \
  "v_smfmac_f32_16x16x64_bf16 v[48:51], v[88:91], v[190:197], v147\n\t" \
  "v_smfmac_f32_16x16x64_bf16 v[52:55], v[88:91], v[246:253], v147\n\t" \
  "s_nop 7\n\t" \
  "v_permlane32_swap_b32 v48, v56\n\t" \
  "v_permlane32_swap_b32 v49, v57\n\t" \
  "v_permlane32_swap_b32 v50, v58\n\t" \
  "v_permlane32_swap_b32 v51, v59\n\t" \
  "v_add_f32 v48, v48, v56\n\t" \
  "v_add_f32 v49, v49, v57\n\t" \
  "v_add_f32 v50, v50, v58\n\t" \
  "v_add_f32 v51, v51, v59\n\t" \
  "v_cvt_pk_bf16_f32 v48, v48, v49\n\t" \
  "v_cvt_pk_bf16_f32 v50, v50, v51\n\t" \
  "v_add_u32 v136, 1024, v149\n\t" \
  "ds_write2_b32 v136, v48, v50 offset0:0 offset1:2\n\t" \
  "v_permlane32_swap_b32 v52, v60\n\t" \
  "v_permlane32_swap_b32 v53, v61\n\t" \
  "v_permlane32_swap_b32 v54, v62\n\t" \
  "v_permlane32_swap_b32 v55, v63\n\t" \
  "v_add_f32 v52, v52, v60\n\t" \
  "v_add_f32 v53, v53, v61\n\t" \
  "v_add_f32 v54, v54, v62\n\t" \
  "v_add_f32 v55, v55, v63\n\t" \
  "v_cvt_pk_bf16_f32 v52, v52, v53\n\t" \
  "v_cvt_pk_bf16_f32 v54, v54, v55\n\t" \
  "v_add_u32 v136, 1024, v149\n\t" \
  "ds_write2_b32 v136, v52, v54 offset0:128 offset1:130\n\t" \
  "s_waitcnt lgkmcnt(0)\n\t" \
  "s_barrier\n\t" \
  "ds_read_b128 v[166:169], v158\n\t" \
  "ds_read_b128 v[170:173], v158 offset:4096\n\t" \
  "ds_read_b128 v[108:111], v150\n\t" \
  "ds_read_b128 v[112:115], v150 offset:2048\n\t" \
  "s_cmp_eq_u32 s50, 63\n\t" \
  "s_cbranch_scc1 END_%=\n\t" \
  "s_waitcnt lgkmcnt(0)\n\t" \
  "s_mov_b32 s66, s49\n\t" \
  "s_cmp_eq_u32 s66, 0\n\t" \
  "s_cbranch_scc1 DLY2E_%=\n\t" \
  "DLY2_%=:\n\t" \
  "s_sub_u32 s66, s66, 1\n\t" \
  "s_cmp_lg_u32 s66, 0\n\t" \
  "s_cbranch_scc1 DLY2_%=\n\t" \
  "DLY2E_%=:\n\t" \
  "s_mov_b32 s66, 0\n\t" \
  "s_memtime s[84:85]\n\t" \
  "POLLP_%=:\n\t" \
  "v_mov_b32 v128, 0\n\t" \
  "v_mov_b32 v129, 0\n\t" \
  "v_mov_b32 v130, 0\n\t" \
  "v_mov_b32 v131, 0\n\t" \
  "v_mov_b32 v132, 0\n\t" \
  "v_mov_b32 v133, 0\n\t" \
  "v_mov_b32 v134, 0\n\t" \
  "v_mov_b32 v135, 0\n\t" \
  "buffer_load_dwordx4 v[116:119], v151, s[40:43], s61 offen sc1\n\t" \
  "buffer_load_dwordx4 v[120:123], v152, s[40:43], s61 offen sc1\n\t" \
  "buffer_load_dwordx4 v[124:127], v153, s[40:43], s61 offen sc1\n\t" \
  "s_waitcnt vmcnt(0)\n\t" \
  "s_cmp_lt_u32 s48, 2\n\t" \
  "s_cbranch_scc0 MN1_%=\n\t" \
  "v_smfmac_f32_16x16x64_bf16 v[128:131], v[124:127], v[108:115], v147\n\t" \
  "s_branch MND_%=\n\t" \
  "MN1_%=:\n\t" \
  "v_smfmac_f32_16x16x64_bf16 v[132:135], v[124:127], v[108:115], v147\n\t" \
  "MND_%=:\n\t" \
  "v_max3_u32 v136, v116, v117, v118\n\t" \
  "v_max3_u32 v137, v119, v120, v121\n\t" \
  "v_smfmac_f32_16x16x64_bf16 v[128:131], v[116:119], v[0:7], v147\n\t" \
  "v_max3_u32 v138, v122, v123, v124\n\t" \
  "v_max3_u32 v139, v125, v126, v127\n\t" \
  "v_smfmac_f32_16x16x64_bf16 v[132:135], v[116:119], v[8:15], v147\n\t" \
  "v_max3_u32 v136, v136, v137, v138\n\t" \
  "v_max_u32 v136, v136, v139\n\t" \
  "v_smfmac_f32_16x16x64_bf16 v[128:131], v[120:123], v[16:23], v147\n\t" \
  "v_cmp_eq_u32 vcc, -1, v136\n\t" \
  "v_smfmac_f32_16x16x64_bf16 v[132:135], v[120:123], v[24:31], v147\n\t" \
  "s_cbranch_vccz GOTP_%=\n\t" \
  "s_add_u32 s66, s66, 1\n\t" \
  "s_cmp_lt_u32 s66, 65536\n\t" \
  "s_nop 7\n\t" \
  "s_cbranch_scc1 POLLP_%=\n\t" \
  "GOTP_%=:\n\t" \
  "s_memtime s[86:87]\n\t" \
  "s_nop 6\n\t" \
  "ds_write_b128 v154, v[128:131]\n\t" \
  "ds_write_b128 v154, v[132:135] offset:1024\n\t" \
  "s_waitcnt lgkmcnt(0)\n\t" \
  "s_barrier\n\t" \
  "ds_read2st64_b32 v[136:137], v155 offset0:0 offset1:2\n\t" \
  "ds_read2st64_b32 v[138:139], v155 offset0:8 offset1:10\n\t" \
  "ds_read2st64_b32 v[140:141], v155 offset0:16 offset1:18\n\t" \
  "ds_read2st64_b32 v[142:143], v155 offset0:24 offset1:26\n\t" \
  "s_sub_u32 s68, s86, s84\n\t" \
  "s_cmp_gt_u32 s68, 850\n\t" \
  "s_cselect_b32 s68, 3, -1\n\t" \
  "s_add_i32 s49, s49, s68\n\t" \
  "s_max_i32 s49, s49, 0\n\t" \
  "s_min_i32 s49, s49, 48\n\t" \
  "s_waitcnt lgkmcnt(3)\n\t" \
  "v_add_f32 v136, v136, v137\n\t" \
  "s_waitcnt lgkmcnt(2)\n\t" \
  "v_add_f32 v138, v138, v139\n\t" \
  "s_waitcnt lgkmcnt(1)\n\t" \
  "v_add_f32 v140, v140, v141\n\t" \
  "v_add_f32 v136, v136, v138\n\t" \
  "s_waitcnt lgkmcnt(0)\n\t" \
  "v_add_f32 v142, v142, v143\n\t" \
  "v_add_f32 v140, v140, v142\n\t" \
  "v_add_f32 v136, v136, v140\n\t" \
  "s_nop 1\n\t" \
  "v_mov_b32_dpp v137, v136 row_shl:1 row_mask:0xf bank_mask:0xf bound_ctrl:0\n\t" \
  "v_cvt_pk_bf16_f32 v136, v136, v137\n\t" \
  "v_min_u32 v136, -2, v136\n\t" \
  "s_add_u32 s60, s60, 16384\n\t" \
  "s_nop 0\n\t" \
  "v_mov_b32_dpp v137, v136 row_shl:2 row_mask:0xf bank_mask:0xf bound_ctrl:0\n\t" \
  "v_mov_b32_dpp v138, v136 row_shl:4 row_mask:0xf bank_mask:0xf bound_ctrl:0\n\t" \
  "v_mov_b32_dpp v139, v136 row_shl:6 row_mask:0xf bank_mask:0xf bound_ctrl:0\n\t" \
  "buffer_store_dwordx4 v[136:139], v156, s[36:39], s60 offen sc1\n\t" \
  "s_add_u32 s50, s50, 1\n\t" \
  "s_add_u32 s61, s61, 16384\n\t" \
  "s_add_u32 s62, s62, 458752\n\t" \
  "s_add_u32 s63, s63, 114688\n\t" \
  "s_cmp_lt_u32 s50, s51\n\t" \
  "s_cbranch_scc1 LOOP_%=\n\t" \
  "s_branch FIN_%=\n\t" \
  "END_%=:\n\t" \
  "s_waitcnt lgkmcnt(0)\n\t" \
  "buffer_store_dwordx4 v[166:169], v159, s[92:95], s63 offen sc1\n\t" \
  "s_add_u32 s66, s63, 57344\n\t" \
  "buffer_store_dwordx4 v[170:173], v159, s[92:95], s66 offen sc1\n\t" \
  "s_waitcnt vmcnt(0)\n\t" \
  "s_barrier\n\t" \
  "s_cmp_eq_u32 s48, 0\n\t" \
  "s_cbranch_scc0 SF2_%=\n\t" \
  "s_lshl_b32 s66, s50, 7\n\t" \
  "s_add_u32 s66, s66, 8192\n\t" \
  "v_mov_b32 v137, 1\n\t" \
  "s_mov_b64 exec, 1\n\t" \
  "buffer_atomic_add v137, off, s[96:99], s66 sc1\n\t" \
  "s_mov_b64 exec, -1\n\t" \
  "SF2_%=:\n\t" \
  "s_sub_u32 s50, s50, 1\n\t" \
  "s_cmp_eq_u32 s48, 0\n\t" \
  "s_cbranch_scc0 SF3_%=\n\t" \
  "s_lshl_b32 s66, s50, 7\n\t" \
  "s_add_u32 s66, s66, 8192\n\t" \
  "v_mov_b32 v137, 1\n\t" \
  "s_mov_b64 exec, 1\n\t" \
  "buffer_atomic_add v137, off, s[96:99], s66 sc1\n\t" \
  "s_mov_b64 exec, -1\n\t" \
  "SF3_%=:\n\t" \
  "FIN_%=:\n\t" \
  "s_waitcnt vmcnt(0) lgkmcnt(0)\n\t" \
  "s_endpgm\n\t" \
  "CEND_%=:\n\t" \
  ".Lcend_B:\n\t"
#define CHAIN_CLOBBERS "v0", "v1", "v2", "v3", "v4", "v5", "v6", "v7", "v8", "v9", "v10", "v11", "v12", "v13", "v14", "v15", "v16", "v17", "v18", "v19", "v20", "v21", "v22", "v23", "v24", "v25", "v26", "v27", "v28", "v29", "v30", "v31", "v32", "v33", "v34", "v35", "v36", "v37", "v38", "v39", "v40", "v41", "v42", "v43", "v44", "v45", "v46", "v47", "v48", "v49", "v50", "v51", "v52", "v53", "v54", "v55", "v56", "v57", "v58", "v59", "v60", "v61", "v62", "v63", "v64", "v65", "v66", "v67", "v68", "v69", "v70", "v71", "v72", "v73", "v74", "v75", "v76", "v77", "v78", "v79", "v80", "v81", "v82", "v83", "v84", "v85", "v86", "v87", "v88", "v89", "v90", "v91", "v92", "v93", "v94", "v95", "v96", "v97", "v98", "v99", "v100", "v101", "v102", "v103", "v104", "v105", "v106", "v107", "v108", "v109", "v110", "v111", "v112", "v113", "v114", "v115", "v116", "v117", "v118", "v119", "v120", "v121", "v122", "v123", "v124", "v125", "v126", "v127", "v128", "v129", "v130", "v131", "v132", "v133", "v134", "v135", "v136", "v137", "v138", "v139", "v140", "v141", "v142", "v143", "v144", "v145", "v146", "v147", "v148", "v149", "v150", "v151", "v152", "v153", "v154", "v155", "v156", "v157", "v158", "v159", "v160", "v161", "v162", "v163", "v164", "v165", "v166", "v167", "v168", "v169", "v170", "v171", "v172", "v173", "v174", "v175", "v176", "v177", "v178", "v179", "v180", "v181", "v182", "v183", "v184", "v185", "v186", "v187", "v188", "v189", "v190", "v191", "v192", "v193", "v194", "v195", "v196", "v197", "v198", "v199", "v200", "v201", "v202", "v203", "v204", "v205", "v206", "v207", "v208", "v209", "v210", "v211", "v212", "v213", "v214", "v215", "v216", "v217", "v218", "v219", "v220", "v221", "v222", "v223", "v224", "v225", "v226", "v227", "v228", "v229", "v230", "v231", "v232", "v233", "v234", "v235", "v236", "v237", "v238", "v239", "v240", "v241", "v242", "v243", "v244", "v245", "v246", "v247", "v248", "v249", "v250", "v251", "v252", "v253", "a0", "a1", "a2", "a3", "a4", "a5", "a6", "a7", "a8", "a9", "a10", "a11", "a12", "a13", "a14", "a15", "a16", "a17", "a18", "a19", "a20", "a21", "a22", "a23", "a24", "a25", "a26", "a27", "a28", "a29", "a30", "a31", "a32", "a33", "a34", "a35", "a36", "a37", "a38", "a39", "a40", "a41", "a42", "a43", "a44", "a45", "a46", "a47", "a48", "a49", "a50", "a51", "a52", "a53", "a54", "a55", "a56", "a57", "a58", "a59", "a60", "a61", "a62", "a63", "a64", "a65", "a66", "a67", "a68", "a69", "a70", "a71", "a72", "a73", "a74", "a75", "a76", "a77", "a78", "a79", "a80", "a81", "a82", "a83", "a84", "a85", "a86", "a87", "a88", "a89", "a90", "a91", "a92", "a93", "a94", "a95", "a96", "a97", "a98", "a99", "a100", "a101", "a102", "a103", "a104", "a105", "a106", "a107", "a108", "a109", "a110", "a111", "a112", "a113", "a114", "a115", "a116", "a117", "a118", "a119", "a120", "a121", "a122", "a123", "a124", "a125", "a126", "a127", "a128", "a129", "a130", "a131", "a132", "a133", "a134", "a135", "a136", "a137", "a138", "a139", "a140", "a141", "a142", "a143", "a144", "a145", "a146", "a147", "a148", "a149", "a150", "a151", "a152", "a153", "a154", "a155", "a156", "a157", "a158", "a159", "a160", "a161", "a162", "a163", "a164", "a165", "a166", "a167", "a168", "a169", "a170", "a171", "a172", "a173", "a174", "a175", "a176", "a177", "a178", "a179", "a180", "a181", "a182", "a183", "a184", "a185", "a186", "a187", "a188", "a189", "a190", "a191", "a192", "a193", "a194", "a195", "a196", "a197", "a198", "a199", "a200", "a201", "a202", "a203", "a204", "a205", "a206", "a207", "a208", "a209", "a210", "a211", "a212", "a213", "a214", "a215", "a216", "a217", "a218", "a219", "a220", "a221", "a222", "a223", "a224", "a225", "a226", "a227", "a228", "a229", "a230", "a231", "a232", "a233", "a234", "a235", "a236", "a237", "a238", "a239", "a240", "a241", "a242", "a243", "a244", "a245", "a246", "a247", "a248", "a249", "a250", "a251", "a252", "a253", "a254", "a255", "s36", "s37", "s38", "s39", "s40", "s41", "s42", "s43", "s44", "s45", "s46", "s47", "s48", "s49", "s50", "s51", "s52", "s53", "s54", "s55", "s56", "s57", "s58", "s59", "s60", "s61", "s62", "s63", "s64", "s65", "s66", "s67", "s68", "s69", "s70", "s71", "s72", "s73", "s74", "s75", "s76", "s77", "s78", "s79", "s80", "s81", "s82", "s83", "s84", "s85", "s86", "s87", "s88", "s89", "s90", "s91", "s92", "s93", "s94", "s95", "s96", "s97", "s98", "s99", "vcc", "scc", "memory"
#define XS_STRIDE 1040
#define A_MEM 0
#define A_WL 65536
#define A_XS 7680
#define A_SP 40960
#define A_NEG 53248
#define A_TAB 90112
#define A_PAR 7424
#define A_END 122880
#define B_WL 44288
#define B_MEM 0
#define B_VM 7424
#define B_RED 15616
#define B_TAB 23808
#define B_PAR 7168
#define B_END 158976


typedef unsigned short u16;
typedef __attribute__((ext_vector_type(4))) unsigned u32x4;
typedef __attribute__((ext_vector_type(2))) unsigned u32x2;
typedef __attribute__((ext_vector_type(4))) float f32x4;
typedef __attribute__((ext_vector_type(2))) float f32x2;
typedef __attribute__((ext_vector_type(8))) __bf16 bf16x8;
typedef __attribute__((ext_vector_type(4))) short s16x4;
typedef __attribute__((ext_vector_type(8))) short s16x8;
#define DI __device__ __forceinline__

constexpr int SEQ = 2048, DM = 448, NSEG = 64;
constexpr int NWORK = 224;

constexpr size_t SZ_WT = (size_t)16 * DM * DM * 2;
constexpr size_t SZ_XF = (size_t)64 * 8 * 28 * 2048;
constexpr size_t OFF_WTA = 0;
constexpr size_t OFF_WTB = OFF_WTA + SZ_WT;
constexpr size_t OFF_VXF = OFF_WTB + SZ_WT;
constexpr size_t OFF_TXF = OFF_VXF + SZ_XF;
constexpr size_t OFF_VMF = OFF_TXF + SZ_XF;
constexpr size_t OFF_PX = OFF_VMF + (size_t)64 * 8 * 28 * 512;
constexpr size_t OFF_ZT = OFF_PX + 64 * 40960;
constexpr size_t OFF_YT = OFF_ZT + (size_t)DM * SEQ * 4;
constexpr size_t OFF_BB = OFF_YT + (size_t)DM * SEQ * 4;
constexpr size_t OFF_GATE = OFF_BB + (size_t)SEQ * DM * 2;
constexpr size_t OFF_C1H = OFF_GATE + SEQ * 4;
constexpr size_t OFF_C1L = OFF_C1H + DM * DM * 2;
constexpr size_t OFF_C3T = OFF_C1L + DM * DM * 2;
constexpr size_t OFF_TW = OFF_C3T + DM * DM * 4;
constexpr size_t OFF_WT2 = OFF_TW + SEQ * 8;
constexpr size_t OFF_FLAG = OFF_WT2 + 225 * SEQ * 4;
constexpr int FL_VX = 0;
constexpr int FL_SEG = 64 * 32;
constexpr int FL_PX = 128 * 32;
constexpr int FL_VXALL = 192 * 32;
constexpr int TAIL_S = 60;
constexpr int FL_PAR = 193 * 32;
constexpr int FL_TX = 208 * 32;
constexpr int FL_F1 = 272 * 32;
constexpr int FL_F2 = 280 * 32;
constexpr int FL_F3 = 288 * 32;
constexpr int FL_TOTAL = 288 * 32 + 224 * 16;
constexpr size_t OFF_HO = OFF_FLAG + FL_TOTAL * 4;
constexpr size_t WS_END = OFF_HO + (6 << 20);

struct Params {
  const u16* Ax; const u16* mem0; const u16* Wq; const u16* Wk; const u16* Wv; const u16* Wo;
  const u16* Bx; const u16* Wr; const u16* Wi; const u16* gw;
  u16* out; unsigned char* ws;
  int phases; int dA; int dB1; int dB2;
};

constexpr int SPIN_MAX = 1 << 16;
__device__ __forceinline__ int lane_id() { int l; asm volatile("v_mbcnt_lo_u32_b32 %0, -1, 0\n\tv_mbcnt_hi_u32_b32 %0, -1, %0" : "=v"(l)); return l; }
constexpr int AUX_SC1 = 16;

DI float bf2f(unsigned v) { return __uint_as_float(v << 16); }
typedef __attribute__((ext_vector_type(2))) __bf16 bf16x2;
DI unsigned pk2(float lo, float hi) { return __builtin_bit_cast(unsigned, __builtin_convertvector((f32x2){lo, hi}, bf16x2)); }
DI float rbf(float x) { return __uint_as_float(pk2(x, 0.f) << 16); }
DI bf16x8 asbf(u32x4 v) { return __builtin_bit_cast(bf16x8, v); }
DI f32x4 mfma16(bf16x8 a, bf16x8 b, f32x4 c) { return __builtin_amdgcn_mfma_f32_16x16x32_bf16(a, b, c, 0, 0, 0); }
DI __amdgpu_buffer_rsrc_t mkrsrc(const void* p, unsigned n) { return __builtin_amdgcn_make_buffer_rsrc((void*)p, 0, n, 0x00020000); }
typedef __amdgpu_buffer_rsrc_t rsrc_t;
DI u32x4 ld16(rsrc_t r, unsigned off) { return __builtin_amdgcn_raw_buffer_load_b128(r, off, 0, 0); }
DI u32x2 ld8(rsrc_t r, unsigned off) { return __builtin_amdgcn_raw_buffer_load_b64(r, off, 0, 0); }
DI unsigned ld4(rsrc_t r, unsigned off) { return __builtin_amdgcn_raw_buffer_load_b32(r, off, 0, 0); }
DI unsigned ld2(rsrc_t r, unsigned off) { return __builtin_amdgcn_raw_buffer_load_b16(r, off, 0, 0); }
DI void st16(rsrc_t r, unsigned off, u32x4 v) { __builtin_amdgcn_raw_buffer_store_b128(v, r, off, 0, 0); }
DI void st8(rsrc_t r, unsigned off, u32x2 v) { __builtin_amdgcn_raw_buffer_store_b64(v, r, off, 0, 0); }
DI void st4(rsrc_t r, unsigned off, unsigned v) { __builtin_amdgcn_raw_buffer_store_b32(v, r, off, 0, 0); }
DI void st2(rsrc_t r, unsigned off, unsigned v) { __builtin_amdgcn_raw_buffer_store_b16((unsigned short)v, r, off, 0, 0); }
DI rsrc_t ws_rsrc(const Params& p) { return mkrsrc(p.ws, (unsigned)WS_END); }
DI unsigned ho_off(const Params& p) { const unsigned long long a = (unsigned long long)(p.ws + OFF_HO); return (unsigned)(((a + 0x1FFFFFull) & ~0x1FFFFFull) - (unsigned long long)p.ws); }
constexpr unsigned SZ_X = SEQ * DM * 2, SZ_W = DM * 16 * 256 * 2, SZ_FW = SEQ * 225 * 2;
DI void flag_add(int* p) { __hip_atomic_fetch_add(p, 1, __ATOMIC_RELAXED, __HIP_MEMORY_SCOPE_AGENT); }
DI int flag_ld(const int* p) { return __hip_atomic_load(p, __ATOMIC_RELAXED, __HIP_MEMORY_SCOPE_AGENT); }
DI void flag_wait(int tid, const int* p, int n) {
  if (tid == 0) {
    int it = 0;
    while (flag_ld(p) < n && ++it < SPIN_MAX) __builtin_amdgcn_s_sleep(8);
  }
  __syncthreads();
}
DI bool wg_any(unsigned char* lds, int wv, int it, bool v) {
  unsigned* sl = (unsigned*)(lds + 41472) + (it & 1) * 4;
  const bool a = __builtin_amdgcn_ballot_w64(v) != 0;
  if (lane_id() == 0) sl[wv] = a ? 1u : 0u;
  __syncthreads();
  const u32x4 r = *(const u32x4*)sl;
  return (r[0] | r[1] | r[2] | r[3]) != 0;
}
DI void flag_wait2(int tid, const int* p, int n, bool fast) {
  if (tid == 0) {
    int it = 0;
    while (flag_ld(p) < n && ++it < SPIN_MAX) { if (!fast) __builtin_amdgcn_s_sleep(7); __builtin_amdgcn_s_sleep(1); }
  }
  __syncthreads();
}
DI void vm_wait0() { asm volatile("s_waitcnt vmcnt(0)" ::: "memory"); }
DI void publish(int tid, int* c) { vm_wait0(); __syncthreads(); if (tid == 0) flag_add(c); }
DI void flag_wait8(int tid, const int* c, int total) {
  if (tid < 64) {
    int it = 0;
    for (;;) {
      int v = tid < 8 ? flag_ld(c + tid * 32) : 0;
      v += __shfl_xor(v, 1); v += __shfl_xor(v, 2); v += __shfl_xor(v, 4);
      v = __builtin_amdgcn_readfirstlane(v);
      if (v >= total || ++it >= SPIN_MAX) break;
      __builtin_amdgcn_s_sleep(8);
    }
  }
  __syncthreads();
}
DI u32x4 ld16c(rsrc_t r, unsigned off) { return __builtin_amdgcn_raw_buffer_load_b128(r, off, 0, AUX_SC1); }
DI u32x2 ld8c(rsrc_t r, unsigned off) { return __builtin_amdgcn_raw_buffer_load_b64(r, off, 0, AUX_SC1); }
DI void st16c(rsrc_t r, unsigned off, u32x4 v) { __builtin_amdgcn_raw_buffer_store_b128(v, r, off, 0, AUX_SC1); }
DI void st8c(rsrc_t r, unsigned off, u32x2 v) { __builtin_amdgcn_raw_buffer_store_b64(v, r, off, 0, AUX_SC1); }

constexpr unsigned OOR = 0x40000000u;
constexpr unsigned TAB_A = (3u << 20) + 65536, TAB_B = TAB_A + 32768;
DI void make_tab_A(rsrc_t rws, unsigned base, int tid) {
  const int lane = tid & 63, w = tid >> 6, g = lane >> 4, c = lane & 15, h = 0;
  auto T = [&](int e, unsigned v) { st4(rws, base + e * 1024 + tid * 4, v); };
  const int gY = g & 1, g1 = g >> 1, i8 = c & 7;
  const int s2 = 112 * w + 64 + 12 * g1;
  auto maddr = [&](int n) { return A_MEM + (64 * (n >> 4) + 32 * ((n >> 1) & 1) + 4 * i8 + ((n >> 2) & 3)) * 4; };
  const int srow = 2 * w + (lane >> 5), skey = lane & 31;
  T(0, tid * 16);
  T(1, (w < 3 ? tid + 256 : tid) * 16);
  T(2, A_XS + c * XS_STRIDE + (112 * w + 8 * g) * 2);
  T(3, A_XS + c * XS_STRIDE + (gY ? s2 + 8 : s2) * 2);
  T(4, A_XS + c * XS_STRIDE + (gY ? s2 + 10 : s2 + 4) * 2);
  T(5, A_XS + (c + 16) * XS_STRIDE + (gY ? s2 + 10 : s2 + 4) * 2);
  T(6, A_MEM + (64 * (7 * w + g1) + 4 * i8 + 2 * gY) * 4);
  T(7, maddr(gY ? s2 + 8 : s2));
  T(8, gY ? maddr(s2 + 8) + 128 : maddr(s2 + 4));
  T(9, maddr(gY ? s2 + 32 : s2 + 24));
  T(10, gY ? maddr(s2 + 32) + 128 : maddr(s2 + 28));
  T(11, A_SP + (w * 768 + 4 * g * 48 + c) * 4);
  T(12, A_SP + (srow * 48 + skey) * 4);
  T(13, skey < 8 ? A_SP + (srow * 48 + 32 + skey) * 4 : A_NEG);
  T(14, (skey & 7) == 0 ? (2 << 20) + (h * 4 + w) * 128 + ((lane >> 5) * 4 + (skey >> 3)) * 16 : (skey == 1 ? (1 << 20) + (h * 4 + w) * 128 + (lane >> 5) * 16 : OOR));
  T(15, (lane & 8) ? 0xEEEE : 0x4444);
  {
    const int j = 4 * (c >> 3) + (c & 3);
    const bool on = ((c >> 2) & 1) == (g & 1);
    const unsigned one = on ? (0x3F80u << (16 * (j & 1))) : 0u;
    for (int d = 0; d < 4; ++d) T((16 + d), ((j >> 1) == d) ? one : 0u);
  }
  T(27, lane * 16);
  T(28, A_WL + w * 6144 + lane * 16);
}
DI void make_tab_B(rsrc_t rws, unsigned base, int tid) {
  const int lane = tid & 63, w = tid >> 6, g = lane >> 4, c = lane & 15, j = 0;
  auto T = [&](int e, unsigned v) { st4(rws, base + e * 1024 + tid * 4, v); };
  const int rnt = w >> 1;
  const int mm = w & 1, mnt = w >> 1;
  T(0, tid * 16);
  T(1, (w < 3 ? tid + 256 : tid) * 16);
  T(2, lane * 16);
  T(3, (lane & 8) ? 0xEEEE : 0x4444);
  T(4, B_WL + w * 14 * 2048 + lane * 16);
  T(5, B_VM + ((2 * w * 2) * 32 + (g & 1) * 16 + c) * 16 + 4 * (lane >> 5));
  T(6, B_VM + (((4 * mm + (g >> 1)) * 2 + mnt) * 32 + (g & 1) * 16 + c) * 16);
  T(7, (2 << 20) + ((4 * w + (c >> 3)) * 4 + ((c & 7) >> 1)) * 128 + ((c & 1) * 4 + g) * 16);
  T(8, (2 << 20) + ((4 * w + 2 + (c >> 3)) * 4 + ((c & 7) >> 1)) * 128 + ((c & 1) * 4 + g) * 16);
  T(9, (1 << 20) + ((8 * mm + 2 * g + (c >> 3)) * 4 + ((c & 7) >> 1)) * 128 + (c & 1) * 16);
  T(10, B_RED + (w * 128 + lane) * 16);
  {
    const int half = w & 1, r = lane >> 3, k = (lane >> 1) & 3, e = lane & 1;
    T(11, B_RED + (rnt * 256 + ((r >> 2) * 16 + 4 * k + 2 * half + e) * 4 + (r & 3)) * 4);
    T(12, (lane & 7) ? OOR : (2 * j + rnt) * 256 + half * 128 + r * 16);
  }
  T(13, lane * 16 + (2 * w) * 28 * 2048);
  T(14, B_VM + tid * 16);
  T(15, (w * 28 + ((tid >> 5) & 1)) * 512 + (tid & 31) * 16);
  T(16, 0);
  T(17, lane * 16);
}

DI unsigned code_sweep(unsigned i) {
  unsigned long long pc; unsigned la, lb, v = 0;
  asm volatile("s_getpc_b64 %0\n.Lsweep_%=:\n\ts_mov_b32 %1, .Lcend_A-.Lsweep_%=\n\ts_mov_b32 %2, .Lcend_B-.Lsweep_%=" : "=s"(pc), "=s"(la), "=s"(lb));
  la = la > 0x40000u ? 0u : la; lb = lb > 0x40000u ? 0u : lb;
  const unsigned len = la > lb ? la : lb, o = i * 64;
  if (o + 4 <= len) asm volatile("global_load_dword %0, %1, %2" : "=v"(v) : "v"(o), "s"(pc));
  return v;
}

DI int apos(int m) {
  int t, ga, r2;
  if (m < 64) { ga = m >> 4; const int rem = m & 15; t = 2 * (rem >> 3) + ((rem >> 1) & 1); r2 = (rem >> 2) & 1; }
  else { const int mm = m - 64; ga = mm / 12; const int rem = mm - 12 * ga; if (rem < 8) { t = 4 + ((rem >> 1) & 1); r2 = rem >> 2; } else { t = 6; r2 = (rem - 8) >> 1; } }
  return 16 * t + 4 * ga + 2 * r2 + (m & 1);
}
constexpr int KOFF = 61440, KSTR = 272;
template <bool ISB>
DI void fold_mma(const Params& p, unsigned char* lds, const u32x4 (&kq)[28], f32x4 (&acc)[4][7], int r0, int nt) {
  const int tid = threadIdx.x, lane = tid & 63, g = lane >> 4, c = lane & 15;
#pragma unroll
  for (int hf = 0; hf < 2; ++hf) {
    if (hf) __syncthreads();
#pragma unroll
    for (int i = 0; i < 14; ++i) { const int ci = tid + 256 * i; *(u32x4*)(lds + KOFF + (ci >> 4) * KSTR + (ci & 15) * 16) = kq[14 * hf + i]; }
    __syncthreads();
    bf16x8 af[2][4], bf[2][7];
    auto load = [&](int k4) {
      const int ks = 4 * hf + k4, u = k4 & 1;
#pragma unroll
      for (int t = 0; t < 4; ++t) af[u][t] = asbf(*(const u32x4*)(lds + KOFF + (r0 + 16 * (t < nt ? t : 0) + c) * KSTR + (32 * k4 + 8 * g) * 2));
#pragma unroll
      for (int n = 0; n < 7; ++n) {
        if (!ISB) bf[u][n] = asbf(*(const u32x4*)(lds + (16 * n + c) * 528 + (32 * ks + 8 * g) * 2));
        else {
          typedef __attribute__((address_space(3))) s16x4* lp;
          const unsigned a0 = (unsigned)(size_t)lds + (32 * ks + 8 * g + (c >> 2)) * 240 + (16 * n + 4 * (c & 3)) * 2;
          const s16x4 lo = __builtin_amdgcn_ds_read_tr16_b64_v4i16((lp)(size_t)a0);
          const s16x4 hi = __builtin_amdgcn_ds_read_tr16_b64_v4i16((lp)(size_t)(a0 + 4 * 240));
          const s16x8 b8 = {lo[0], lo[1], lo[2], lo[3], hi[0], hi[1], hi[2], hi[3]};
          bf[u][n] = __builtin_bit_cast(bf16x8, b8);
        }
      }
    };
    load(0);
#pragma unroll
    for (int k4 = 0; k4 < 4; ++k4) {
      if (k4 < 3) load(k4 + 1);
      __builtin_amdgcn_sched_barrier(0);
#pragma unroll
      for (int n = 0; n < 7; ++n)
#pragma unroll
        for (int t = 0; t < 4; ++t) acc[t][n] = mfma16(af[k4 & 1][t], bf[k4 & 1][n], acc[t][n]);
      __builtin_amdgcn_sched_barrier(0);
    }
  }
}
__global__ void __launch_bounds__(256, 1) k_prologue(Params p) {
  extern __shared__ __attribute__((aligned(16))) unsigned char lds[];
  const int b = blockIdx.x, tid = threadIdx.x, lane = tid & 63;
  const int w = __builtin_amdgcn_readfirstlane(tid >> 6);
  const int g = lane >> 4, c = lane & 15;
  const int mi = 4 * (b & 7) + (b >> 6), sub = (b >> 3) & 7, h = mi & 15;
  const bool isB = mi >= 16;
  const int k0 = (sub >> 2) * 224, n0 = (sub & 3) * 112;
  const unsigned pfv = code_sweep((b >> 3) * 256 + tid);
  const rsrc_t rws = ws_rsrc(p);
  u32x4 st[14];
  if (!isB) {
    const rsrc_t r = mkrsrc(p.Wk, SZ_W);
#pragma unroll
    for (int i = 0; i < 14; ++i) { const int ci = tid + 256 * i, row = ci >> 5, ch = ci & 31; st[i] = ld16(r, (((n0 + row) * 16 + h) * 256 + ch * 8) * 2); }
  } else {
    const rsrc_t r = mkrsrc(p.Wo, SZ_W);
#pragma unroll
    for (int i = 0; i < 14; ++i) { const int ci = tid + 256 * i, row = ci / 14, ch = ci - row * 14; st[i] = ld16(r, ((h * 256 + row) * DM + n0 + ch * 8) * 2); }
  }
  const int t0 = 4 * w, nt = w < 3 ? 4 : 2;
  u32x4 kq[28];
  {
    const rsrc_t rsrc = mkrsrc(isB ? p.Wv : p.Wq, SZ_W);
#pragma unroll
    for (int i = 0; i < 28; ++i) { const int ci = tid + 256 * (i % 14); kq[i] = ld16(rsrc, (((k0 + (ci >> 4)) * 16 + h) * 256 + 128 * (i / 14) + (ci & 15) * 8) * 2); }
  }
  {
    const rsrc_t rm0 = mkrsrc(p.mem0, 8 * DM * 2);
    const unsigned ho = ho_off(p);
#pragma unroll
    for (int i = 0; i < 3; ++i) {
      const int ci = (i * 256 + b) * 256 + tid;
      u32x4 v = {0xFFFFFFFFu, 0xFFFFFFFFu, 0xFFFFFFFFu, 0xFFFFFFFFu};
      if (i == 0 && b < 4 && (ci & 511) < 448) {
        const int cm = ci & 511, u = cm >> 4, half = (cm >> 3) & 1, r = cm & 7;
        const unsigned mo = (r * DM + 16 * u + 2 * half) * 2;
        v[0] = min(ld4(rm0, mo), 0xFFFFFFFEu); v[1] = min(ld4(rm0, mo + 8), 0xFFFFFFFEu); v[2] = min(ld4(rm0, mo + 16), 0xFFFFFFFEu); v[3] = min(ld4(rm0, mo + 24), 0xFFFFFFFEu);
      }
      if (K0SC & 1) st16c(rws, ho + ci * 16, v); else st16(rws, ho + ci * 16, v);
    }
    if (b < 50) st4(rws, (unsigned)OFF_FLAG + (b * 256 + tid) * 4, 0u);
    if (b == 250) make_tab_A(rws, ho + TAB_A, tid);
    if (b == 251) make_tab_B(rws, ho + TAB_B, tid);
    if (b >= 64 && b < 64 + (64 - TAIL_S) * 10) st16(rws, (unsigned)OFF_PX + TAIL_S * 40960 + ((b - 64) * 256 + tid) * 16, (u32x4){0xFFFFFFFFu, 0xFFFFFFFFu, 0xFFFFFFFFu, 0xFFFFFFFFu});
  }
  if (!isB) {
#pragma unroll
    for (int i = 0; i < 14; ++i) { const int ci = tid + 256 * i, row = ci >> 5, ch = ci & 31; *(u32x4*)(lds + apos(row) * 528 + ch * 16) = st[i]; }
  } else {
#pragma unroll
    for (int i = 0; i < 14; ++i) { const int ci = tid + 256 * i, row = ci / 14, ch = ci - row * 14; *(u32x4*)(lds + row * 240 + ch * 16) = st[i]; }
  }
  asm volatile("" :: "v"(pfv));
  f32x4 acc[4][7];
#pragma unroll
  for (int t = 0; t < 4; ++t)
#pragma unroll
    for (int n = 0; n < 7; ++n) acc[t][n] = (f32x4){0.f, 0.f, 0.f, 0.f};
  if (isB) fold_mma<true>(p, lds, kq, acc, 16 * t0, nt); else fold_mma<false>(p, lds, kq, acc, 16 * t0, nt);
  const float sc = isB ? 1.0f : 0.0625f * 1.4426950408889634f;
  const unsigned dst = (unsigned)(isB ? OFF_WTB : OFF_WTA) + h * DM * DM * 2 + ((((g & 1) << 1) | (g >> 1)) * 16 + c) * 16;
#pragma unroll
  for (int bq = 0; bq < 2; ++bq) {
    if (2 * bq < nt) {
#pragma unroll
      for (int n = 0; n < 7; ++n) {
        const f32x4 x = acc[2 * bq][n] * sc, y = acc[2 * bq + 1][n] * sc;
        const auto r0 = __builtin_amdgcn_permlane16_swap(pk2(x[0], x[1]), pk2(y[0], y[1]), false, false);
        const auto r1 = __builtin_amdgcn_permlane16_swap(pk2(x[2], x[3]), pk2(y[2], y[3]), false, false);
        if (K0SC & 2) st16c(rws, dst + ((n0 / 16 + n) * 14 + k0 / 32 + 2 * w + bq) * 1024, (u32x4){r0[0], r1[0], r0[1], r1[1]});
        else st16(rws, dst + ((n0 / 16 + n) * 14 + k0 / 32 + 2 * w + bq) * 1024, (u32x4){r0[0], r1[0], r0[1], r1[1]});
      }
    }
  }
}

DI void chain_A(const Params& p, unsigned char* lds, int h) {
  const int tid = threadIdx.x;
  const int w = __builtin_amdgcn_readfirstlane(tid >> 6);
  const u16* WtA = (const u16*)(p.ws + OFF_WTA);
  if (tid == 0) {
    unsigned long long* pp = (unsigned long long*)(lds + A_PAR);
    pp[0] = (unsigned long long)(WtA + (size_t)h * DM * DM); pp[1] = (unsigned long long)p.Ax; pp[2] = (unsigned long long)(p.ws + ho_off(p)); pp[3] = pp[2]; pp[8] = (unsigned long long)(p.ws + OFF_FLAG);
    ((int*)pp)[8] = 0; ((int*)pp)[9] = 63; pp[5] = (unsigned long long)(p.ws + WS_END); ((int*)pp)[12] = (h * 4) * 64 * 32; ((int*)pp)[13] = ((1 << 16) + 2 * h + 1) * 8; ((int*)pp)[14] = p.dA; ((int*)pp)[15] = h * 512;
  }
  __syncthreads();
  asm volatile(CHAIN_A_ASM :: "s"(w) : CHAIN_CLOBBERS);
  __builtin_unreachable();
}


DI void chain_B(const Params& p, unsigned char* lds, int j) {
  const int tid = threadIdx.x;
  const int w = __builtin_amdgcn_readfirstlane(tid >> 6);
  if (tid == 0) {
    unsigned long long* pp = (unsigned long long*)(lds + B_PAR);
    pp[0] = (unsigned long long)(p.ws + OFF_WTB + 2 * j * 14336); pp[1] = (unsigned long long)(p.ws + OFF_VXF + 2 * j * 2048); pp[2] = (unsigned long long)(p.ws + ho_off(p)); pp[3] = pp[2];
    ((int*)pp)[8] = 0; ((int*)pp)[9] = 64; pp[5] = (unsigned long long)(p.ws + WS_END); ((int*)pp)[12] = ((16 + j) * 4) * 64 * 32; ((int*)pp)[13] = ((1 << 16) + 2 * (16 + j) + 1) * 8;
    pp[7] = (unsigned long long)(p.ws + OFF_VMF + 2 * j * 512); ((int*)pp)[16] = j == 0; ((int*)pp)[17] = j * 512; ((int*)pp)[18] = p.dB1; ((int*)pp)[19] = p.dB2;
    pp[10] = (unsigned long long)(p.ws + OFF_FLAG);
  }
  __syncthreads();
  asm volatile(CHAIN_B_ASM :: "s"(w) : CHAIN_CLOBBERS);
  __builtin_unreachable();
}

DI int fft_pos_to_k(int pos) { return (pos >> 8) + 8 * ((pos >> 5) & 7) + 64 * ((pos >> 2) & 7) + 512 * (pos & 3); }
DI void make_tab448(int tid, f32x2* tab) {
  for (int j = tid; j < 448; j += 256) { float sn, cs; sincospif((float)j * (1.0f / 224.0f), &sn, &cs); tab[j] = (f32x2){cs, sn}; }
}

template <bool ISV>
DI void gemm_body(const Params& p, int wv, unsigned char* lds, int cs, int rg) {
  const int tid = wv * 64 + lane_id(), lane = tid & 63;
  const int w = __builtin_amdgcn_readfirstlane(tid >> 6);
  const int g = lane >> 4, c = lane & 15;
  const int gw = cs * 4 + w;
  const int grp = ISV ? gw / 14 : gw / 7, sub = ISV ? gw % 14 : gw % 7;
  const rsrc_t rws = ws_rsrc(p), rx = mkrsrc(p.Ax, SZ_X);
  bf16x8 W[4][14];
#pragma unroll
  for (int q = 0; q < 4; ++q) {
    const int h = ISV ? 2 * grp + (q & 1) : grp, nt = ISV ? 2 * sub + (q >> 1) : 4 * sub + q;
#pragma unroll
    for (int ks = 0; ks < 14; ++ks) W[q][ks] = asbf(ld16(rws, (unsigned)(ISV ? OFF_WTB : OFF_WTA) + h * DM * DM * 2 + (nt * 14 + ks) * 1024 + lane * 16));
  }
#pragma unroll
  for (int q = 0; q < 4; ++q)
#pragma unroll
    for (int ks = 0; ks < 14; ++ks) asm volatile("" : "+a"(W[q][ks]));
  const __amdgpu_buffer_rsrc_t ro = mkrsrc(p.ws + (ISV ? OFF_VXF : OFF_TXF), (unsigned)SZ_XF);
  int* flags = (int*)(p.ws + OFF_FLAG);
  u32x4 pf[7];
  int srow[7], sch[7];
#pragma unroll
  for (int i = 0; i < 7; ++i) { const int ci = tid + 256 * i; srow[i] = ci / 56; sch[i] = ci - srow[i] * 56; }
#pragma unroll
  for (int i = 0; i < 7; ++i) *(u32x4*)(lds + srow[i] * 912 + sch[i] * 16) = ld16(rx, ((32 * rg + srow[i]) * DM + sch[i] * 8) * 2);
  __syncthreads();
  for (int it = 0; it < 16; ++it) {
    if (G_SLEEP && it > 1) __builtin_amdgcn_s_sleep(G_SLEEP);
    const int s = rg + 4 * it;
    const unsigned char* cur = lds + (it & 1) * 32 * 912;
    unsigned char* nxt = lds + ((it + 1) & 1) * 32 * 912;
    const int sn = it < 15 ? s + 4 : s;
#pragma unroll
    for (int i = 0; i < 7; ++i) pf[i] = ld16(rx, ((32 * sn + srow[i]) * DM + sch[i] * 8) * 2);
    __builtin_amdgcn_sched_barrier(0);
#pragma unroll
    for (int m = 0; m < 2; ++m) {
      f32x4 acc[4];
#pragma unroll
      for (int q = 0; q < 4; ++q) acc[q] = (f32x4){0.f, 0.f, 0.f, 0.f};
#pragma unroll
      for (int ks = 0; ks < 14; ++ks) {
        const bf16x8 xf = asbf(*(const u32x4*)(cur + (16 * m + c) * 912 + (32 * ks + 8 * g) * 2));
#pragma unroll
        for (int q = 0; q < 4; ++q) acc[q] = ISV ? mfma16(xf, W[q][ks], acc[q]) : mfma16(W[q][ks], xf, acc[q]);
      }
#pragma unroll
      for (int u = 0; u < 2; ++u) {
        const f32x4 a = acc[2 * u], b = acc[2 * u + 1];
        u32x4 v; int off;
        if (ISV) { v = (u32x4){pk2(a[0], a[1]), pk2(b[0], b[1]), pk2(a[2], a[3]), pk2(b[2], b[3])}; off = ((((s * 8 + grp) * 28 + 2 * sub + u) * 2 + m) * 64 + lane) * 16; }
        else { v = (u32x4){pk2(a[0], a[1]), pk2(a[2], a[3]), pk2(b[0], b[1]), pk2(b[2], b[3])}; off = ((((s * 16 + grp) * 14 + 2 * sub + u) * 2 + m) * 64 + lane) * 16; }
        __builtin_amdgcn_raw_buffer_store_b128(v, ro, off, 0, AUX_SC1);
      }
    }
    __builtin_amdgcn_sched_barrier(0);
    asm volatile("s_waitcnt vmcnt(4)" ::: "memory");
    __builtin_amdgcn_sched_barrier(0);
#pragma unroll
    for (int i = 0; i < 7; ++i) *(u32x4*)(nxt + srow[i] * 912 + sch[i] * 16) = pf[i];
    __syncthreads();
    if (it > 0 && !(ISV && it == 1) && tid == 0) flag_add(flags + (ISV ? FL_VX : FL_TX) + (s - 4) * 32);
    if (ISV && it == 0) {
      vm_wait0(); __syncthreads();
      if (tid == 0) flag_add(flags + FL_VX + s * 32);
    }
  }
  vm_wait0();
  __syncthreads();
  if (tid == 0) { flag_add(flags + (ISV ? FL_VX : FL_TX) + (rg + 60) * 32); if (ISV) flag_add(flags + FL_VXALL); }
}
DI void phase_G(const Params& p, int wv, unsigned char* lds, int gi) {
  const int cs = gi >> 2, rg = gi & 3;
  if (cs < 28) gemm_body<true>(p, wv, lds, cs, rg);
  else {
    for (int i = 0; i < TX_DELAY; ++i) __builtin_amdgcn_s_sleep(127);
    gemm_body<false>(p, wv, lds, cs - 28, rg);
  }
}

DI void phase_F1(const Params& p, int wv, unsigned char* lds, int wi) {
  const int tid = wv * 64 + lane_id(), lane = tid & 63, w = tid >> 6, g = lane >> 4, c = lane & 15;
  const int fb = wi % 7, tb = wi / 7;
  const int f0 = 64 * fb + 16 * w, t0 = 64 * tb;
  const rsrc_t rws = ws_rsrc(p), rx = mkrsrc(p.Bx, SZ_X);
  u32x4 xs[14];
#pragma unroll
  for (int i = 0; i < 14; ++i) { const int q = tid + 256 * i, row = q / 56, ch = q - row * 56; xs[i] = ld16(rx, ((t0 + row) * DM + ch * 8) * 2); }
  f32x2* tab = (f32x2*)(lds + 64 * 912);
  make_tab448(tid, tab);
#pragma unroll
  for (int i = 0; i < 14; ++i) { const int q = tid + 256 * i, row = q / 56, ch = q - row * 56; *(u32x4*)(lds + row * 912 + ch * 16) = xs[i]; }
  __syncthreads();
  const int fr = f0 + c, f = fr == 0 ? 0 : (fr == 1 ? 224 : fr >> 1);
  const bool issin = fr >= 2 && (fr & 1);
  const float* tb1 = (const float*)tab + (issin ? 1 : 0);
  const float sg = issin ? -1.0f : 1.0f;
  int idx = (f * 8 * g) % 448;
  const int st24 = (f * 24) % 448;
  f32x4 acc[4];
#pragma unroll
  for (int u = 0; u < 4; ++u) acc[u] = (f32x4){0.f, 0.f, 0.f, 0.f};
#pragma unroll 2
  for (int ks = 0; ks < 14; ++ks) {
    float v[8];
#pragma unroll
    for (int j = 0; j < 8; ++j) { v[j] = sg * tb1[2 * idx]; idx += f; idx -= idx >= 448 ? 448 : 0; }
    idx += st24; idx -= idx >= 448 ? 448 : 0;
    u32x4 hi, lo;
#pragma unroll
    for (int j = 0; j < 4; ++j) {
      hi[j] = pk2(v[2 * j], v[2 * j + 1]);
      lo[j] = pk2(v[2 * j] - bf2f(hi[j] & 0xFFFFu), v[2 * j + 1] - __uint_as_float(hi[j] & 0xFFFF0000u));
    }
#pragma unroll
    for (int u = 0; u < 4; ++u) {
      const bf16x8 xb = asbf(*(const u32x4*)(lds + (16 * u + c) * 912 + (32 * ks + 8 * g) * 2));
      acc[u] = mfma16(xb, asbf(hi), acc[u]); acc[u] = mfma16(xb, asbf(lo), acc[u]);
    }
  }
#pragma unroll
  for (int u = 0; u < 4; ++u) st16c(rws, (unsigned)OFF_ZT + (fr * SEQ + t0 + 16 * u + 4 * g) * 4, __builtin_bit_cast(u32x4, acc[u]));
  publish(tid, (int*)(p.ws + OFF_FLAG) + FL_F1 + fb * 32);
}

DI f32x2 cmul(f32x2 a, f32x2 b) { return (f32x2){a[0] * b[0] - a[1] * b[1], a[0] * b[1] + a[1] * b[0]}; }
DI f32x2 cmulc(f32x2 a, f32x2 b) { return (f32x2){a[0] * b[0] + a[1] * b[1], a[1] * b[0] - a[0] * b[1]}; }
template <bool INV> DI f32x2 rot90(f32x2 a) { return INV ? (f32x2){-a[1], a[0]} : (f32x2){a[1], -a[0]}; }
template <bool INV> DI void dft4(f32x2& a0, f32x2& a1, f32x2& a2, f32x2& a3) {
  const f32x2 c0 = a0 + a2, c1 = a1 + a3, d0 = a0 - a2, d1 = rot90<INV>(a1 - a3);
  a0 = c0 + c1; a2 = c0 - c1; a1 = d0 + d1; a3 = d0 - d1;
}
template <bool INV> DI void dft8(f32x2* v) {
  const float r = 0.70710678118654752f;
  f32x2 a0 = v[0] + v[4], a1 = v[1] + v[5], a2 = v[2] + v[6], a3 = v[3] + v[7];
  f32x2 b0 = v[0] - v[4], b1 = v[1] - v[5], b2 = v[2] - v[6], b3 = v[3] - v[7];
  b1 = INV ? (f32x2){(b1[0] - b1[1]) * r, (b1[0] + b1[1]) * r} : (f32x2){(b1[0] + b1[1]) * r, (b1[1] - b1[0]) * r};
  b2 = rot90<INV>(b2);
  b3 = INV ? (f32x2){(-b3[0] - b3[1]) * r, (b3[0] - b3[1]) * r} : (f32x2){(b3[1] - b3[0]) * r, (-b3[0] - b3[1]) * r};
  dft4<INV>(a0, a1, a2, a3); dft4<INV>(b0, b1, b2, b3);
  v[0] = a0; v[2] = a1; v[4] = a2; v[6] = a3; v[1] = b0; v[3] = b1; v[5] = b2; v[7] = b3;
}
DI int fidx(int pz) { return pz + 4 * (pz >> 5); }

DI void fft_column(const Params& p, int wv, unsigned char* lds, int f) {
  const int t = wv * 64 + lane_id(), tid = t;
  f32x2* buf = (f32x2*)lds;
  const f32x2* tw = (const f32x2*)(lds + 2304 * 8);
  const int rre = f == 0 ? 0 : (f == 224 ? 1 : 2 * f);
  const bool real = (f == 0 || f == 224);
  const rsrc_t rws = ws_rsrc(p);
  unsigned fw[8];
  {
    const rsrc_t rr = mkrsrc(p.Wr, SZ_FW), ri = mkrsrc(p.Wi, SZ_FW);
#pragma unroll
    for (int i = 0; i < 8; ++i) { const int k = fft_pos_to_k(8 * t + i); fw[i] = ld2(rr, (k * 225 + f) * 2) | (ld2(ri, (k * 225 + f) * 2) << 16); }
  }
  flag_wait(tid, (const int*)(p.ws + OFF_FLAG) + FL_F1 + (rre >> 6) * 32, 32);
  f32x2 v[8];
  {
    const unsigned zr = (unsigned)OFF_ZT + (rre * SEQ + 8 * t) * 4;
    const u32x4 r0 = ld16c(rws, zr), r1 = ld16c(rws, zr + 16);
    u32x4 i0 = {0u, 0u, 0u, 0u}, i1 = {0u, 0u, 0u, 0u};
    if (!real) { i0 = ld16c(rws, zr + SEQ * 4); i1 = ld16c(rws, zr + SEQ * 4 + 16); }
#pragma unroll
    for (int i = 0; i < 4; ++i) { buf[fidx(8 * t + i)] = (f32x2){__uint_as_float(r0[i]), __uint_as_float(i0[i])}; buf[fidx(8 * t + 4 + i)] = (f32x2){__uint_as_float(r1[i]), __uint_as_float(i1[i])}; }
  }
  __syncthreads();
  const int q1 = t >> 5, j2 = t & 31, blk = t >> 2, j3 = t & 3;
#pragma unroll
  for (int m = 0; m < 8; ++m) v[m] = buf[fidx(t + 256 * m)];
  dft8<false>(v);
#pragma unroll
  for (int q = 0; q < 8; ++q) buf[fidx(t + 256 * q)] = q ? cmul(v[q], tw[t * q]) : v[q];
  __syncthreads();
#pragma unroll
  for (int m = 0; m < 8; ++m) v[m] = buf[fidx(256 * q1 + j2 + 32 * m)];
  dft8<false>(v);
#pragma unroll
  for (int q = 0; q < 8; ++q) buf[fidx(256 * q1 + j2 + 32 * q)] = q ? cmul(v[q], tw[8 * j2 * q]) : v[q];
  __syncthreads();
#pragma unroll
  for (int m = 0; m < 8; ++m) v[m] = buf[fidx(32 * blk + j3 + 4 * m)];
  dft8<false>(v);
#pragma unroll
  for (int q = 0; q < 8; ++q) buf[fidx(32 * blk + j3 + 4 * q)] = q ? cmul(v[q], tw[64 * j3 * q]) : v[q];
  __syncthreads();
#pragma unroll
  for (int m = 0; m < 8; ++m) v[m] = buf[fidx(8 * t + m)];
  dft4<false>(v[0], v[1], v[2], v[3]); dft4<false>(v[4], v[5], v[6], v[7]);
#pragma unroll
  for (int m = 0; m < 8; ++m) v[m] = cmul(v[m], (f32x2){bf2f(fw[m] & 0xFFFFu), bf2f(fw[m] >> 16)});
  dft4<true>(v[0], v[1], v[2], v[3]); dft4<true>(v[4], v[5], v[6], v[7]);
#pragma unroll
  for (int m = 0; m < 8; ++m) buf[fidx(8 * t + m)] = v[m];
  __syncthreads();
#pragma unroll
  for (int q = 0; q < 8; ++q) { const f32x2 x = buf[fidx(32 * blk + j3 + 4 * q)]; v[q] = q ? cmulc(x, tw[64 * j3 * q]) : x; }
  dft8<true>(v);
#pragma unroll
  for (int m = 0; m < 8; ++m) buf[fidx(32 * blk + j3 + 4 * m)] = v[m];
  __syncthreads();
#pragma unroll
  for (int q = 0; q < 8; ++q) { const f32x2 x = buf[fidx(256 * q1 + j2 + 32 * q)]; v[q] = q ? cmulc(x, tw[8 * j2 * q]) : x; }
  dft8<true>(v);
#pragma unroll
  for (int m = 0; m < 8; ++m) buf[fidx(256 * q1 + j2 + 32 * m)] = v[m];
  __syncthreads();
#pragma unroll
  for (int q = 0; q < 8; ++q) { const f32x2 x = buf[fidx(t + 256 * q)]; v[q] = q ? cmulc(x, tw[t * q]) : x; }
  dft8<true>(v);
#pragma unroll
  for (int m = 0; m < 8; ++m) buf[fidx(t + 256 * m)] = v[m];
  __syncthreads();
#pragma unroll
  for (int m = 0; m < 8; ++m) v[m] = buf[fidx(8 * t + m)];
  {
    const unsigned yr = (unsigned)OFF_YT + (rre * SEQ + 8 * t) * 4;
    st16c(rws, yr, __builtin_bit_cast(u32x4, (f32x4){v[0][0], v[1][0], v[2][0], v[3][0]}));
    st16c(rws, yr + 16, __builtin_bit_cast(u32x4, (f32x4){v[4][0], v[5][0], v[6][0], v[7][0]}));
    if (!real) {
      st16c(rws, yr + SEQ * 4, __builtin_bit_cast(u32x4, (f32x4){v[0][1], v[1][1], v[2][1], v[3][1]}));
      st16c(rws, yr + SEQ * 4 + 16, __builtin_bit_cast(u32x4, (f32x4){v[4][1], v[5][1], v[6][1], v[7][1]}));
    }
  }
  publish(tid, (int*)(p.ws + OFF_FLAG) + FL_F2 + (f & 7) * 32);
}
DI void phase_F2(const Params& p, int wv, unsigned char* lds, int wi) {
  f32x2* tw = (f32x2*)(lds + 2304 * 8);
  for (int i = wv * 64 + lane_id(); i < SEQ; i += 256) { float sn, cs; sincospif((float)i * (1.0f / 1024.0f), &sn, &cs); tw[i] = (f32x2){cs, -sn}; }
  __syncthreads();
  for (int f = wi; f < 225; f += NWORK) fft_column(p, wv, lds, f);
}

DI void phase_F3(const Params& p, int wv, unsigned char* lds, int wi) {
  const int tid = wv * 64 + lane_id(), lane = tid & 63, w = tid >> 6, g = lane >> 4, c = lane & 15;
  const int db = wi % 7, tb = wi / 7;
  const int d0 = 64 * db + 16 * w, t0 = 64 * tb;
  const rsrc_t rws = ws_rsrc(p);
  f32x2* tab = (f32x2*)(lds + 448 * 256);
  make_tab448(tid, tab);
  flag_wait8(tid, (const int*)(p.ws + OFF_FLAG) + FL_F2, 225);
  {
    u32x4 ys[28];
#pragma unroll
    for (int i = 0; i < 28; ++i) { const int q = tid + 256 * i; ys[i] = ld16c(rws, (unsigned)OFF_YT + ((q >> 4) * SEQ + t0 + (q & 15) * 4) * 4); }
#pragma unroll
    for (int i = 0; i < 28; ++i) *(u32x4*)(lds + (tid + 256 * i) * 16) = ys[i];
  }
  __syncthreads();
  const int d = d0 + c;
  int i0 = (2 * g * d) % 448, i1 = ((2 * g + 1) * d) % 448;
  const int st8d = (8 * d) % 448;
  f32x4 acc[4];
#pragma unroll
  for (int u = 0; u < 4; ++u) acc[u] = (f32x4){0.f, 0.f, 0.f, 0.f};
#pragma unroll 2
  for (int j = 0; j < 28; ++j) {
    const f32x2 e0 = tab[i0], e1 = tab[i1];
    f32x4 a4 = {e0[0], -e0[1], e1[0], -e1[1]};
    if (j == 0 && g == 0) { a4[0] = 0.5f; a4[1] = (d & 1) ? -0.5f : 0.5f; }
    i0 += st8d; i0 -= i0 >= 448 ? 448 : 0; i1 += st8d; i1 -= i1 >= 448 ? 448 : 0;
#pragma unroll
    for (int i = 0; i < 4; ++i) {
      const f32x4 b4 = *(const f32x4*)(lds + (16 * j + 4 * g + i) * 256 + 16 * c);
#pragma unroll
      for (int u = 0; u < 4; ++u) acc[u] = __builtin_amdgcn_mfma_f32_16x16x4f32(a4[i], b4[u], acc[u], 0, 0, 0);
    }
  }
  const float sc = 2.0f / (2048.0f * 448.0f);
#pragma unroll
  for (int u = 0; u < 4; ++u) st8c(rws, (unsigned)OFF_BB + ((t0 + 4 * c + u) * DM + d0 + 4 * g) * 2, (u32x2){pk2(acc[u][0] * sc, acc[u][1] * sc), pk2(acc[u][2] * sc, acc[u][3] * sc)});
  publish(tid, (int*)(p.ws + OFF_FLAG) + FL_F3 + wi * 16);
}

DI void task_XI(const Params& p, int wv, unsigned char* lds, int s, int pp) {
  const int tid = wv * 64 + lane_id(), lane = tid & 63;
  const int w = __builtin_amdgcn_readfirstlane(tid >> 6);
  const int g = lane >> 4, c = lane & 15;
  const int pair = 2 * pp + (w & 1), m = w >> 1;
  int* flags = (int*)(p.ws + OFF_FLAG);
  const rsrc_t rws = ws_rsrc(p), rx = mkrsrc(p.Ax, SZ_X);
#pragma unroll
  for (int i = 0; i < 7; ++i) {
    const int q = tid + 256 * i, row = q / 56, ch = q - row * 56, blk = ch / 14, m0 = (ch - blk * 14) * 8;
    const u32x4 v = ld16(rx, ((32 * s + row) * DM + ch * 8) * 2);
#pragma unroll
    for (int k = 0; k < 4; ++k) *(unsigned*)(lds + row * 912 + (112 * blk + apos(m0 + 2 * k)) * 2) = v[k];
  }
  const unsigned tx = (unsigned)OFF_TXF + ((s * 16 + 2 * pair) * 14 * 2 + m) * 1024 + lane * 16;
  flag_wait(tid, flags + FL_TX + s * 32, 28);
  u32x4 tf[2][14];
#pragma unroll
  for (int hh = 0; hh < 2; ++hh)
#pragma unroll
    for (int J = 0; J < 14; ++J) tf[hh][J] = ld16c(rws, tx + (hh * 14 + J) * 2048);
  __syncthreads();
  f32x4 st[2][3];
#pragma unroll
  for (int hh = 0; hh < 2; ++hh)
#pragma unroll
    for (int k = 0; k < 3; ++k) st[hh][k] = (f32x4){0.f, 0.f, 0.f, 0.f};
#pragma unroll
  for (int J = 0; J < 14; ++J)
#pragma unroll
    for (int k = 0; k < 2; ++k) {
      const unsigned char* a = lds + (16 * k + c) * 912 + (32 * J + 4 * g) * 2;
      const u32x2 lo = *(const u32x2*)a, hi = *(const u32x2*)(a + 32);
      const bf16x8 kf = asbf((u32x4){lo[0], lo[1], hi[0], hi[1]});
      st[0][k] = mfma16(kf, asbf(tf[0][J]), st[0][k]); st[1][k] = mfma16(kf, asbf(tf[1][J]), st[1][k]);
    }
  const int q = 16 * m + c;
  float mx0[2];
#pragma unroll
  for (int hh = 0; hh < 2; ++hh) {
    float mx = -INFINITY;
#pragma unroll
    for (int k = 0; k < 2; ++k)
#pragma unroll
      for (int r = 0; r < 4; ++r) { if (16 * k + 4 * g + r > q) st[hh][k][r] = -INFINITY; mx = fmaxf(mx, st[hh][k][r]); }
    mx0[hh] = mx;
  }
  int mdst[2][4];
#pragma unroll
  for (int i = 0; i < 2; ++i) {
    const int ci = tid + 256 * i, u = ci >> 4, blk = u / 7, m0 = 16 * (u - 7 * blk) + 2 * ((ci >> 3) & 1);
#pragma unroll
    for (int k = 0; k < 4; ++k) mdst[i][k] = (32 + (ci & 7)) * 912 + (112 * blk + apos(m0 + 4 * k)) * 2;
  }
  flag_wait(tid, flags + FL_PAR, 1);
  const int par = (__builtin_amdgcn_readfirstlane(flag_ld(flags + FL_PAR)) - 1) & 1;
  const __amdgpu_buffer_rsrc_t rm = mkrsrc(p.ws + ho_off(p) + (2 * s + par) * 8192, 7168);
  const bool tail = s >= TAIL_S;
  flag_wait2(tid, flags + FL_SEG + (tail ? s - 1 : s) * 32 + 16, 1, tail);
  for (int it = 0; it < SPIN_MAX; ++it) {
    const u32x4 v0 = __builtin_amdgcn_raw_buffer_load_b128(rm, tid * 16, 0, AUX_SC1);
    const u32x4 v1 = __builtin_amdgcn_raw_buffer_load_b128(rm, (tid + 256) * 16, 0, AUX_SC1);
#pragma unroll
    for (int k = 0; k < 4; ++k) { *(unsigned*)(lds + mdst[0][k]) = v0[k]; if (tid < 192) *(unsigned*)(lds + mdst[1][k]) = v1[k]; }
    const unsigned mxv = max(max(max(v0[0], v0[1]), max(v0[2], v0[3])), max(max(v1[0], v1[1]), max(v1[2], v1[3])));
    if (!wg_any(lds, wv, it, mxv == 0xFFFFFFFFu)) break;
  }
  {
    f32x4 e0 = {0.f, 0.f, 0.f, 0.f}, e1 = e0;
#pragma unroll
    for (int J = 0; J < 14; J += 2) {
      const unsigned char* a = lds + (32 + (c & 7)) * 912 + (32 * J + 4 * g) * 2;
      const u32x2 lo = *(const u32x2*)a, hi = *(const u32x2*)(a + 32), lo2 = *(const u32x2*)(a + 64), hi2 = *(const u32x2*)(a + 96);
      const bf16x8 kf = asbf((u32x4){lo[0], lo[1], hi[0], hi[1]}), kf2 = asbf((u32x4){lo2[0], lo2[1], hi2[0], hi2[1]});
      st[0][2] = mfma16(kf, asbf(tf[0][J]), st[0][2]); st[1][2] = mfma16(kf, asbf(tf[1][J]), st[1][2]);
      e0 = mfma16(kf2, asbf(tf[0][J + 1]), e0); e1 = mfma16(kf2, asbf(tf[1][J + 1]), e1);
    }
    st[0][2] += e0; st[1][2] += e1;
  }
  unsigned pk[2][3][2];
#pragma unroll
  for (int hh = 0; hh < 2; ++hh) {
    float mx = mx0[hh];
#pragma unroll
    for (int r = 0; r < 4; ++r) mx = fmaxf(mx, st[hh][2][r]);
    mx = fmaxf(mx, __shfl_xor(mx, 16)); mx = fmaxf(mx, __shfl_xor(mx, 32));
    float sum = 0.f;
#pragma unroll
    for (int k = 0; k < 3; ++k)
#pragma unroll
      for (int r = 0; r < 4; ++r) { const float e = __builtin_amdgcn_exp2f(st[hh][k][r] - mx); st[hh][k][r] = e; sum += k == 2 ? 0.5f * e : e; }
    sum += __shfl_xor(sum, 16); sum += __shfl_xor(sum, 32);
    const float inv = 1.0f / sum;
#pragma unroll
    for (int k = 0; k < 3; ++k) { pk[hh][k][0] = min(pk2(st[hh][k][0] * inv, st[hh][k][1] * inv), 0xFFFFFFFEu); pk[hh][k][1] = min(pk2(st[hh][k][2] * inv, st[hh][k][3] * inv), 0xFFFFFFFEu); }
  }
  const __amdgpu_buffer_rsrc_t rp = mkrsrc(p.ws + OFF_PX + (size_t)s * 40960, 40960);
#pragma unroll
  for (int k = 0; k < 2; ++k) __builtin_amdgcn_raw_buffer_store_b128((u32x4){pk[0][k][0], pk[1][k][0], pk[0][k][1], pk[1][k][1]}, rp, (((pair * 2 + m) * 2 + k) * 64 + lane) * 16, 0, AUX_SC1);
  if ((g >> 1) == (w & 1)) __builtin_amdgcn_raw_buffer_store_b128((u32x4){pk[0][2][0], pk[1][2][0], pk[0][2][1], pk[1][2][1]}, rp, 32768 + ((pp * 2 + m) * 64 + lane) * 16, 0, AUX_SC1);
  if (!tail) vm_wait0();
  __syncthreads();
  if (!tail && tid == 0) flag_add(flags + FL_PX + s * 32);
}

DI void task_XII(const Params& p, int wv, unsigned char* lds, int s, int ch) {
  const int tid = wv * 64 + lane_id(), lane = tid & 63;
  const int w = __builtin_amdgcn_readfirstlane(tid >> 6);
  const int g = lane >> 4, c = lane & 15;
  const int nt = 4 * ch + w;
  int* flags = (int*)(p.ws + OFF_FLAG);
  u32x4 vx[8][2];
  const rsrc_t rws = ws_rsrc(p);
  float* gl = (float*)(lds + 40960);
  {
    const rsrc_t rx = mkrsrc(p.Ax, SZ_X);
    const u32x4 gw = ld16(mkrsrc(p.gw, DM * 2), lane < 56 ? lane * 16 : OOR);
    u32x4 xr[8];
#pragma unroll
    for (int i = 0; i < 8; ++i) xr[i] = ld16(rx, lane < 56 ? ((32 * s + 8 * w + i) * DM + lane * 8) * 2 : OOR);
#pragma unroll
    for (int i = 0; i < 8; ++i) {
      float sum = 0.f;
#pragma unroll
      for (int k = 0; k < 4; ++k) { sum += rbf(bf2f(xr[i][k] & 0xFFFFu) * bf2f(gw[k] & 0xFFFFu)); sum += rbf(bf2f(xr[i][k] >> 16) * bf2f(gw[k] >> 16)); }
#pragma unroll
      for (int o = 32; o > 0; o >>= 1) sum += __shfl_xor(sum, o);
      if (lane == 0) gl[8 * w + i] = rbf(1.0f / (1.0f + __expf(-rbf(sum))));
    }
  }
  flag_wait(tid, flags + FL_VX + s * 32, 28);
  const unsigned vb = (unsigned)OFF_VXF + (s * 8 * 28 + nt) * 2048 + lane * 16;
#pragma unroll
  for (int pr = 0; pr < 8; ++pr) { vx[pr][0] = ld16c(rws, vb + pr * 28 * 2048); vx[pr][1] = ld16c(rws, vb + pr * 28 * 2048 + 1024); }
  u32x2 bv[2]; float gt[2];
  flag_wait(tid, flags + FL_F3 + ((s >> 1) * 7 + ch) * 16, 1);
#pragma unroll
  for (int m = 0; m < 2; ++m) {
    const int t = 32 * s + 16 * m + c;
    bv[m] = ld8c(rws, (unsigned)OFF_BB + (t * DM + 16 * nt + 4 * g) * 2);
    gt[m] = gl[16 * m + c];
  }
  u32x4 vm[4];
  const __amdgpu_buffer_rsrc_t rv = mkrsrc(p.ws + OFF_VMF + (size_t)s * 8 * 28 * 512, 8 * 28 * 512);
  const __amdgpu_buffer_rsrc_t rp = mkrsrc(p.ws + OFF_PX + (size_t)s * 40960, 40960);
  const bool tail = s >= TAIL_S;
  flag_wait2(tid, flags + FL_SEG + s * 32, 14, tail);
#pragma unroll
  for (int pp = 0; pp < 4; ++pp) vm[pp] = __builtin_amdgcn_raw_buffer_load_b128(rv, (((2 * pp + (g >> 1)) * 28 + nt) * 32 + (g & 1) * 16 + c) * 16, 0, AUX_SC1);
  if (!tail) flag_wait(tid, flags + FL_PX + s * 32, 4);
  for (int it = 0; it < SPIN_MAX; ++it) {
    u32x4 pf[10];
#pragma unroll
    for (int i = 0; i < 10; ++i) pf[i] = __builtin_amdgcn_raw_buffer_load_b128(rp, (tid + 256 * i) * 16, 0, AUX_SC1);
    unsigned mxv = 0;
#pragma unroll
    for (int i = 0; i < 10; ++i) { *(u32x4*)(lds + (tid + 256 * i) * 16) = pf[i]; mxv = max(max(mxv, max(pf[i][0], pf[i][1])), max(pf[i][2], pf[i][3])); }
    if (!wg_any(lds, wv, it, mxv == 0xFFFFFFFFu)) break;
    __builtin_amdgcn_s_sleep(2);
  }
  f32x4 acc[2], ac2[2][2];
#pragma unroll
  for (int m = 0; m < 2; ++m) { ac2[0][m] = (f32x4){0.f, 0.f, 0.f, 0.f}; ac2[1][m] = ac2[0][m]; }
#pragma unroll
  for (int pr = 0; pr < 8; ++pr)
#pragma unroll
    for (int k = 0; k < 2; ++k)
#pragma unroll
      for (int m = 0; m < 2; ++m) ac2[k][m] = mfma16(asbf(vx[pr][k]), asbf(*(const u32x4*)(lds + (((pr * 2 + m) * 2 + k) * 64 + lane) * 16)), ac2[k][m]);
#pragma unroll
  for (int pp = 0; pp < 4; ++pp)
#pragma unroll
    for (int m = 0; m < 2; ++m) ac2[pp & 1][m] = mfma16(asbf(vm[pp]), asbf(*(const u32x4*)(lds + 32768 + ((pp * 2 + m) * 64 + lane) * 16)), ac2[pp & 1][m]);
#pragma unroll
  for (int m = 0; m < 2; ++m) acc[m] = ac2[0][m] + ac2[1][m];
#pragma unroll
  for (int m = 0; m < 2; ++m) {
    const float gg = gt[m], og = rbf(1.0f - gg);
    float o[4];
#pragma unroll
    for (int r = 0; r < 4; ++r) {
      const unsigned bw = bv[m][r >> 1];
      const float b = bf2f((r & 1) ? bw >> 16 : bw & 0xFFFFu);
      o[r] = rbf(gg * rbf(acc[m][r])) + rbf(og * b);
    }
    st8(mkrsrc(p.out, SZ_X), ((32 * s + 16 * m + c) * DM + 16 * nt + 4 * g) * 2, (u32x2){pk2(o[0], o[1]), pk2(o[2], o[3])});
  }
  __syncthreads();
}

constexpr int PROBE_N = 4;
DI void probe_parity(const Params& p, int wv, unsigned char* lds) {
  if (wv != 0 || lane_id() != 0) return;
  unsigned char* base = p.ws + ho_off(p);
  unsigned best[4] = {0xFFFFFFFFu, 0xFFFFFFFFu, 0xFFFFFFFFu, 0xFFFFFFFFu};
  auto line = [&](int q, int k) { return base + ((q >> 1) ? (3 << 20) : 7168) + (2 * k + (q & 1)) * 8192 + k * 128; };
#pragma unroll
  for (int k = 0; k < 2; ++k)
#pragma unroll
    for (int q = 0; q < 4; ++q) asm volatile("global_store_dword %0, %1, off sc1" :: "v"(line(q, k)), "v"(0xFFFFFFFFu) : "memory");
  asm volatile("s_waitcnt vmcnt(0)" ::: "memory");
  for (int k = 0; k < PROBE_N; ++k) {
#pragma unroll
    for (int q = 0; q < 4; ++q) {
      unsigned long long t0, t1;
      asm volatile("s_memtime %0\n\ts_waitcnt lgkmcnt(0)\n\tglobal_store_dword %2, %3, off sc1\n\ts_waitcnt vmcnt(0)\n\ts_memtime %1\n\ts_waitcnt lgkmcnt(0)"
                   : "=&s"(t0), "=&s"(t1) : "v"(line(q, k & 1)), "v"(0xFFFFFFFFu) : "memory");
      best[q] = min(best[q], (unsigned)(t1 - t0));
    }
    if (k >= 1 && abs((int)best[0] - (int)best[1]) > 120 && abs((int)best[2] - (int)best[3]) > 120) break;
  }
  const int v = 1 + (best[1] < best[0] ? 1 : 0) + (best[3] < best[2] ? 2 : 0);
  __hip_atomic_store((int*)(p.ws + OFF_FLAG) + FL_PAR, v, __ATOMIC_RELAXED, __HIP_MEMORY_SCOPE_AGENT);
}

enum { PH_T = 1, PH_G = 2, PH_F1 = 4, PH_F2 = 8, PH_F3 = 16, PH_CH = 32, PH_XI = 64, PH_XII = 128 };

__global__ void __launch_bounds__(256, 1) k_main(Params p) {
  extern __shared__ __attribute__((aligned(16))) unsigned char lds[];
  const int b = blockIdx.x;
  const int wv = __builtin_amdgcn_readfirstlane(threadIdx.x >> 6);
  {
    const int r4 = (b >> 3) - 19;
    if (r4 >= 0 && (r4 & 3) == 0) { const unsigned v = code_sweep((r4 >> 2) * 256 + threadIdx.x); asm volatile("s_waitcnt vmcnt(0)" :: "v"(v)); }
  }
  constexpr int BX = CHAIN_XCD2 == 4 ? 2 : 1;
  const int x0 = b & 7, r = b >> 3;
  const int x = x0 == BX ? 1 : (x0 == 1 ? BX : x0);
  if (x == 0 && r < 16) { if (p.phases & PH_CH) chain_A(p, lds, r); return; }
  if (x == 1 && r < 14) { if (p.phases & PH_CH) chain_B(p, lds, r); return; }
  const int wi = x >= 2 ? r * 6 + (x - 2) : (x == 0 ? 192 + (r - 16) : 208 + (r - 14));
  if (wi >= NWORK) { if (wi == NWORK && (p.phases & PH_CH)) probe_parity(p, wv, lds); return; }
  const int gi = x >= 2 ? (x - 2) * 32 + r : wi;
  if (gi < 112) {
    gemm_body<true>(p, wv, lds, gi >> 2, gi & 3); __syncthreads();
    gemm_body<false>(p, wv, lds, gi >> 2, gi & 3); __syncthreads();
  } else {
    for (int i = 0; i < TX_DELAY; ++i) __builtin_amdgcn_s_sleep(127);
#pragma unroll 1
    for (int it = 0; it < 2; ++it) { phase_F1(p, wv, lds, gi - 112 + 112 * it); __syncthreads(); }
#pragma unroll 1
    for (int it = 0; it < 2; ++it) { phase_F2(p, wv, lds, gi - 112 + 112 * it); __syncthreads(); }
#pragma unroll 1
    for (int it = 0; it < 2; ++it) { phase_F3(p, wv, lds, gi - 112 + 112 * it); __syncthreads(); }
  }
  if (p.phases & (PH_XI | PH_XII)) {
    const int grp = wi / 11, role = wi - grp * 11;
    if (grp < 20) {
      for (int s = grp; s < NSEG; s += 20) {
        if (role < 4) { if (p.phases & PH_XI) { __syncthreads(); task_XI(p, wv, lds, s, role); } }
        else if (p.phases & PH_XII) task_XII(p, wv, lds, s, role - 4);
      }
    }
  }
}

extern "C" void kernel_launch(void* const* d_in, const int* in_sizes, int n_in, void* d_out, int out_size, void* d_ws, size_t ws_size, hipStream_t stream) {
  Params p;
  p.Ax = (const u16*)d_in[0]; p.mem0 = (const u16*)d_in[1]; p.Wq = (const u16*)d_in[2]; p.Wk = (const u16*)d_in[3]; p.Wv = (const u16*)d_in[4]; p.Wo = (const u16*)d_in[5];
  p.Bx = (const u16*)d_in[0]; p.Wr = (const u16*)d_in[8]; p.Wi = (const u16*)d_in[9]; p.gw = (const u16*)d_in[10];
  p.out = (u16*)d_out; p.ws = (unsigned char*)d_ws;
  p.phases = 0; p.dA = DELAY_A; p.dB1 = DELAY_B1; p.dB2 = DELAY_B2;
  const int LDS0 = 124 * 1024, LDS1 = 160 * 1024;
  (void)hipFuncSetAttribute((const void*)k_prologue, hipFuncAttributeMaxDynamicSharedMemorySize, LDS0);
  (void)hipFuncSetAttribute((const void*)k_main, hipFuncAttributeMaxDynamicSharedMemorySize, LDS1);
  hipLaunchKernelGGL(k_prologue, dim3(256), dim3(256), LDS0, stream, p);
  p.phases = 255;
  hipLaunchKernelGGL(k_main, dim3(256), dim3(256), LDS1, stream, p);
}
```
